```python
import torch

def _softmax(x, axis=-1):
    return torch.softmax(x, dim=axis)

def _gelu(x):
    return torch.nn.functional.gelu(x, approximate='tanh')

def _silu(x):
    return torch.nn.functional.silu(x)

def _sigmoid(x):
    return torch.sigmoid(x)

def _layernorm(x, axis=-1, eps=1e-05):
    m = x.mean(dim=axis, keepdim=True)
    v = x.var(dim=axis, keepdim=True, unbiased=False)
    return (x - m) / torch.sqrt(v + eps)

def _scatter_add(tgt, idx, src, axis=0):
    return tgt.clone().index_add_(axis, idx, src)

def _segment_sum(src, seg, n):
    out = torch.zeros((n,) + src.shape[1:], dtype=src.dtype)
    return out.index_add_(0, seg, src)

def _pad(x, pw):
    flat = tuple((v for pair in reversed(pw) for v in pair))
    return torch.nn.functional.pad(x, flat)
kernel_name = 'adaLN_cond__memory_aug_recurrent_mem_tr__glu_gated_swiglu__prompt'

def setup_inputs():

    _w = lambda a: (lambda t: t.to(torch.bfloat16) if t.is_floating_point() else t)(torch.as_tensor(a))

    def _A_setup():
        B, S, Dm = (1, 2048, 640)
        H, Dh = (32, 192)
        M, seg = (8, 32)
        seg = min(seg, S)
        S = S // seg * seg
        return {'x': _w((lambda *s: torch.randn(*s, dtype=torch.bfloat16) * 0.05)(B, S, Dm)), 'mem0': _w((lambda *s: torch.randn(*s, dtype=torch.bfloat16) * 0.8)(B, M, Dm)), 'Wq': _w((lambda *s: torch.randn(*s, dtype=torch.bfloat16) * 0.8)(Dm, H, Dh) * Dm ** (-0.5)), 'Wk': _w((lambda *s: torch.randn(*s, dtype=torch.bfloat16) * 0.8)(Dm, H, Dh) * Dm ** (-0.5)), 'Wv': _w((lambda *s: torch.randn(*s, dtype=torch.bfloat16) * 0.8)(Dm, H, Dh) * Dm ** (-0.5)), 'Wo': _w((lambda *s: torch.randn(*s, dtype=torch.bfloat16) * 0.8)(H, Dh, Dm) * (H * Dh) ** (-0.5)), 'seg_len': seg}
    _A_in = _A_setup()

    def _B_setup():
        B, S, D, F = (1, 2048, 640, 1536)
        return {'x': _w((lambda *s: torch.randn(*s, dtype=torch.bfloat16) * 0.05)(B, S, D)), 'w1': _w((lambda *s: torch.randn(*s, dtype=torch.bfloat16) * 0.4)(D, F) * (2.0 / D) ** 0.5), 'w3': _w((lambda *s: torch.randn(*s, dtype=torch.bfloat16) * 0.4)(D, F) * (2.0 / D) ** 0.5), 'w2': _w((lambda *s: torch.randn(*s, dtype=torch.bfloat16) * 0.4)(F, D) * (2.0 / F) ** 0.5)}
    _B_in = _B_setup()
    return {'A_x': _A_in['x'], 'A_mem0': _A_in['mem0'], 'A_Wq': _A_in['Wq'], 'A_Wk': _A_in['Wk'], 'A_Wv': _A_in['Wv'], 'A_Wo': _A_in['Wo'], 'A_seg_len': _A_in['seg_len'], 'B_x': _B_in['x'], 'B_w1': _B_in['w1'], 'B_w3': _B_in['w3'], 'B_w2': _B_in['w2'], 'c': _w((lambda *s: torch.randn(*s, dtype=torch.bfloat16) * 0.1)(1, 640) * 8.0), 'w_mod': _w((lambda *s: torch.randn(*s, dtype=torch.bfloat16) * 0.1)(640, 6 * 640) * 640 ** (-0.5) * 8.0)}

def reference(A_x, A_mem0, A_Wq, A_Wk, A_Wv, A_Wo, A_seg_len, B_x, B_w1, B_w3, B_w2, c, w_mod):

    def _A_ref(x, mem0, Wq, Wk, Wv, Wo, seg_len):
        B, S, Dm = x.shape
        H, Dh = (Wq.shape[1], Wq.shape[2])
        M = mem0.shape[1]
        scale = Dh ** (-0.5)
        n_seg = S // seg_len
        L = M + seg_len
        idx = torch.arange(L)
        mask = (idx[:, None] < M) | (idx[:, None] >= idx[None, :])
        mem = mem0
        outs = []
        for s in range(n_seg):
            xs = x[:, s * seg_len:(s + 1) * seg_len, :].clone()
            h = torch.concatenate([mem, xs], axis=1)
            q = torch.einsum('btd,dhe->bhte', h, Wq)
            k = torch.einsum('btd,dhe->bhte', h, Wk)
            v = torch.einsum('btd,dhe->bhte', h, Wv)
            scores = torch.einsum('bhqe,bhke->bhqk', q, k) * scale
            scores = torch.where(mask[None, None], scores, -1000000000.0)
            p = _softmax(scores, axis=-1)
            o = torch.einsum('bhqk,bhke->bhqe', p, v)
            o = torch.einsum('bhte,hed->btd', o, Wo)
            mem = o[:, :M, :].clone()
            outs.append(o[:, M:, :])
        return torch.concatenate(outs, axis=1)

    def _B_ref(x, w1, w3, w2):
        gate = torch.einsum('bsd,df->bsf', x, w1)
        up = torch.einsum('bsd,df->bsf', x, w3)
        h = _silu(gate) * up
        out = torch.einsum('bsf,fd->bsd', h, w2)
        return out
    x = A_x
    mod = (_silu(c) @ w_mod).reshape(c.shape[0], 6, 1, -1)
    sa, ca, ga, sb, cb, gb = (mod[:, 0].clone(), mod[:, 1].clone(), mod[:, 2].clone(), mod[:, 3].clone(), mod[:, 4].clone(), mod[:, 5].clone())
    h = _layernorm(x, axis=-1) * (1 + ca) + sa
    x = x + ga * _A_ref(h, A_mem0, A_Wq, A_Wk, A_Wv, A_Wo, A_seg_len)
    h = _layernorm(x, axis=-1) * (1 + cb) + sb
    x = x + gb * _B_ref(h, B_w1, B_w3, B_w2)
    return x
```

```cpp
#include <hip/hip_runtime.h>
#include <hip/hip_bf16.h>
#include <stdint.h>

constexpr int S_ = 2048, D_ = 640, H_ = 32, DH_ = 192, M_ = 8, SEG_ = 32, NSEG_ = 64, F_ = 1536;
constexpr int HD_ = H_ * DH_;
constexpr int NU_ = 80;
constexpr int UROWS_ = S_ + D_;
constexpr int UCH_ = UROWS_ / 8;

typedef short bf16x8 __attribute__((ext_vector_type(8)));
typedef short bf16x4 __attribute__((ext_vector_type(4)));
typedef float f32x4 __attribute__((ext_vector_type(4)));
typedef unsigned v4u __attribute__((ext_vector_type(4)));
typedef unsigned v2u __attribute__((ext_vector_type(2)));
typedef unsigned short u16;
typedef short v4i16_t __attribute__((ext_vector_type(4)));

__device__ __forceinline__ float bf2f(u16 x) { return __uint_as_float(((unsigned)x) << 16); }
__device__ __forceinline__ u16 f2bf(float f) { return __builtin_bit_cast(u16, (__bf16)f); }
__device__ __forceinline__ float rbf(float f) { return bf2f(f2bf(f)); }
__device__ __forceinline__ unsigned pack2(float a, float b) { return (unsigned)f2bf(a) | ((unsigned)f2bf(b) << 16); }
__device__ __forceinline__ unsigned canon2(unsigned x) {
  if ((x & 0xFFFFu) == 0xFFFFu) x &= 0xFFFFFFFEu;
  if ((x >> 16) == 0xFFFFu) x &= 0xFFFEFFFFu;
  return x;
}
__device__ __forceinline__ bool has_sent(v4u v) {
  bool b = false;
#pragma unroll
  for (int i = 0; i < 4; ++i) { unsigned x = v[i]; b |= ((x & 0xFFFFu) == 0xFFFFu) | ((x >> 16) == 0xFFFFu); }
  return b;
}

constexpr size_t al256(size_t x) { return (x + 255) & ~(size_t)255; }
constexpr size_t WS_TMO  = 0;
constexpr size_t WS_MEMB = 256;
constexpr size_t WS_PB   = WS_MEMB + (size_t)65 * 10240;
constexpr size_t WS_SENT_END = WS_PB + (size_t)64 * 20480;
constexpr size_t WS_MOD  = al256(WS_SENT_END);
constexpr size_t WS_H1   = al256(WS_MOD + 3840 * 4);
constexpr size_t WS_Q    = al256(WS_H1 + (size_t)S_ * D_ * 2);
constexpr size_t WS_K    = WS_Q + (size_t)S_ * HD_ * 2;
constexpr size_t WS_V    = WS_K + (size_t)S_ * HD_ * 2;
constexpr size_t WS_U    = WS_V + (size_t)S_ * HD_ * 2;
constexpr size_t WS_WT   = WS_U + (size_t)H_ * UCH_ * D_ * 8 * 2;
constexpr size_t WS_KM   = WS_WT + (size_t)2 * D_ * HD_ * 2;
constexpr size_t WS_UM   = WS_KM + (size_t)NSEG_ * 8 * HD_ * 2;
constexpr size_t WS_PALL = WS_UM + (size_t)NSEG_ * H_ * D_ * 8 * 2;
constexpr size_t WS_X1   = WS_PALL + (size_t)S_ * 1280 * 2;
constexpr size_t WS_H2   = WS_X1 + (size_t)S_ * D_ * 2;
constexpr size_t WS_G    = WS_H2 + (size_t)S_ * D_ * 2;
constexpr size_t WS_END  = WS_G + (size_t)S_ * F_ * 2;

__global__ void __launch_bounds__(256) k_mod(const u16* __restrict__ c, const u16* __restrict__ wmod, float* __restrict__ mod) {
  __shared__ float red[4][64];
  const int lane = threadIdx.x & 63, w = threadIdx.x >> 6;
  const int n = blockIdx.x * 64 + lane;
  float acc = 0.f;
  for (int k = w * 160; k < w * 160 + 160; ++k) {
    float cv = bf2f(c[k]);
    float sv = rbf(cv / (1.f + __expf(-cv)));
    acc += sv * bf2f(wmod[(size_t)k * 3840 + n]);
  }
  red[w][lane] = acc;
  __syncthreads();
  if (w == 0) mod[n] = rbf(red[0][lane] + red[1][lane] + red[2][lane] + red[3][lane]);
}

__global__ void __launch_bounds__(256) k_ln(const u16* __restrict__ x, const float* __restrict__ shift, const float* __restrict__ scale, u16* __restrict__ y) {
  const int lane = threadIdx.x & 63, w = threadIdx.x >> 6;
  const int row = blockIdx.x * 4 + w;
  const unsigned* xr = (const unsigned*)(x + (size_t)row * D_);
  float v[10];
  float s = 0.f;
#pragma unroll
  for (int i = 0; i < 5; ++i) { unsigned u = xr[lane + 64 * i]; v[2 * i] = bf2f((u16)(u & 0xFFFF)); v[2 * i + 1] = bf2f((u16)(u >> 16)); s += v[2 * i] + v[2 * i + 1]; }
#pragma unroll
  for (int m = 32; m >= 1; m >>= 1) s += __shfl_xor(s, m);
  const float mean = s * (1.f / D_);
  float q = 0.f;
#pragma unroll
  for (int i = 0; i < 10; ++i) { float d = v[i] - mean; q += d * d; }
#pragma unroll
  for (int m = 32; m >= 1; m >>= 1) q += __shfl_xor(q, m);
  const float mb = rbf(mean), vb = rbf(q * (1.f / D_));
  const float den = rbf(sqrtf(rbf(vb + 1e-5f)));
  unsigned* yr = (unsigned*)(y + (size_t)row * D_);
#pragma unroll
  for (int i = 0; i < 5; ++i) {
    int c = 2 * (lane + 64 * i);
    float o[2];
#pragma unroll
    for (int e = 0; e < 2; ++e) {
      float t = rbf(rbf(v[2 * i + e] - mb) / den);
      t = rbf(t * rbf(1.f + scale[c + e]));
      o[e] = t + shift[c + e];
    }
    yr[lane + 64 * i] = pack2(o[0], o[1]);
  }
}

__global__ void __launch_bounds__(256) k_prep(const u16* __restrict__ mem0, const u16* __restrict__ Wq, const u16* __restrict__ Wk, u16* __restrict__ memb, u16* __restrict__ wt) {
  const int gid = blockIdx.x * 256 + threadIdx.x;
  if (gid < 640) {
    int u = gid >> 3, i = gid & 7;
    *(v4u*)(memb + gid * 8) = *(const v4u*)(mem0 + i * D_ + u * 8);
  }
  const int lane = gid & 63; int f = gid >> 6;
  const int ks = f % 20; f /= 20; const int p = f % 6; f /= 6; const int w = f & 3; const int h = f >> 2;
  if (h >= H_) return;
  const u16* W = (p < 3) ? Wq : Wk;
  const int et = p % 3;
  const int e = (3 * w + et) * 16 + (lane & 15);
  const int k0 = ks * 32 + 8 * (lane >> 4);
  u16 v[8];
#pragma unroll
  for (int j = 0; j < 8; ++j) v[j] = W[(size_t)(k0 + j) * HD_ + h * DH_ + e];
  v4u o = {v[0] | ((unsigned)v[1] << 16), v[2] | ((unsigned)v[3] << 16), v[4] | ((unsigned)v[5] << 16), v[6] | ((unsigned)v[7] << 16)};
  *(v4u*)(wt + (size_t)gid * 8) = o;
}

__device__ __forceinline__ int fswz(int r) { return (((r >> 1) & 1) << 1) | (((r >> 3) & 1) << 2); }

template <int BM, int BN, bool SWAP, class PT>
__device__ __forceinline__ void gemm_tile(const PT& P, int m0, int n0, char* smem, f32x4 (&acc)[BM / 32][BN / 32]) {
  constexpr int ABYTES = BM * 128, BBYTES = BN * 128, STG = ABYTES + BBYTES;
  constexpr int NA = BM * 8 / 256, NB = BN * 8 / 256;
  const int tid = threadIdx.x, lane = tid & 63, wid = tid >> 6, wm = wid >> 1, wn = wid & 1;
  const int K = P.K;
  const u16* asrc[NA];
  const u16* bsrc[NB];
#pragma unroll
  for (int it = 0; it < NA; ++it) { int p = it * 256 + tid; int row = p >> 3, slot = p & 7; asrc[it] = P.a_row(m0 + row) + ((slot ^ (row & 7)) * 8); }
#pragma unroll
  for (int it = 0; it < NB; ++it) { int p = it * 256 + tid; int sub = p >> 9, r = (p >> 3) & 63, slot = p & 7; bsrc[it] = P.b_col(n0, sub) + (size_t)r * P.ldb + ((slot ^ fswz(r)) * 8); }
  auto stage = [&](int kt, int buf) {
    char* base = smem + buf * STG;
#pragma unroll
    for (int it = 0; it < NA; ++it)
      __builtin_amdgcn_global_load_lds((const unsigned*)(asrc[it] + kt * 64), (unsigned*)(base + (it * 256 + tid) * 16), 16, 0, 0);
#pragma unroll
    for (int it = 0; it < NB; ++it)
      __builtin_amdgcn_global_load_lds((const unsigned*)(bsrc[it] + (size_t)kt * 64 * P.ldb), (unsigned*)(base + ABYTES + (it * 256 + tid) * 16), 16, 0, 0);
  };
#pragma unroll
  for (int i = 0; i < BM / 32; ++i)
#pragma unroll
    for (int j = 0; j < BN / 32; ++j) acc[i][j] = (f32x4){0.f, 0.f, 0.f, 0.f};
  const int nk = K / 64;
  const int fr = lane & 15, g = lane >> 4, q = fr >> 2, pp = lane & 3;
  stage(0, 0);
  __syncthreads();
  for (int kt = 0; kt < nk; ++kt) {
    const int cur = kt & 1;
    if (kt + 1 < nk) stage(kt + 1, cur ^ 1);
    char* abase = smem + cur * STG;
    char* bbase = abase + ABYTES;
#pragma unroll
    for (int kk = 0; kk < 2; ++kk) {
      bf16x8 af[BM / 32], bf[BN / 32];
#pragma unroll
      for (int i = 0; i < BM / 32; ++i) {
        int row = wm * (BM / 2) + i * 16 + fr;
        af[i] = *(const bf16x8*)(abase + row * 128 + (((kk * 4 + g) ^ (row & 7)) * 16));
      }
#pragma unroll
      for (int j = 0; j < BN / 32; ++j) {
        int col = wn * (BN / 2) + j * 16;
        int sub = col >> 6, c0 = (col & 63) >> 3;
        v4i16_t lo, hi;
        { int r = kk * 32 + 8 * g + q; lo = __builtin_amdgcn_ds_read_tr16_b64_v4i16((__attribute__((address_space(3))) v4i16_t*)(bbase + sub * 8192 + r * 128 + (((c0 + (pp >> 1)) ^ fswz(r)) * 16) + 8 * (pp & 1))); }
        { int r = kk * 32 + 8 * g + 4 + q; hi = __builtin_amdgcn_ds_read_tr16_b64_v4i16((__attribute__((address_space(3))) v4i16_t*)(bbase + sub * 8192 + r * 128 + (((c0 + (pp >> 1)) ^ fswz(r)) * 16) + 8 * (pp & 1))); }
        bf[j] = (bf16x8){lo[0], lo[1], lo[2], lo[3], hi[0], hi[1], hi[2], hi[3]};
      }
#pragma unroll
      for (int i = 0; i < BM / 32; ++i)
#pragma unroll
        for (int j = 0; j < BN / 32; ++j)
          acc[i][j] = SWAP ? __builtin_amdgcn_mfma_f32_16x16x32_bf16(bf[j], af[i], acc[i][j], 0, 0, 0)
                           : __builtin_amdgcn_mfma_f32_16x16x32_bf16(af[i], bf[j], acc[i][j], 0, 0, 0);
    }
    __syncthreads();
  }
}

struct PQKV {
  const u16* A; const u16* W; int K; int ldb;
  __device__ const u16* a_row(int m) const { return A + (size_t)m * D_; }
  __device__ const u16* b_col(int n0, int sub) const { return W + (n0 % HD_) + sub * 64; }
};
__global__ void __launch_bounds__(256, 2) k_qkv(const u16* __restrict__ h1, const u16* __restrict__ Wq, const u16* __restrict__ Wk, const u16* __restrict__ Wv, u16* __restrict__ out) {
  extern __shared__ __attribute__((aligned(16))) char smem[];
  constexpr int BM = 128, BN = 192;
  const int nb = blockIdx.x % 96, mb = blockIdx.x / 96;
  const int m0 = mb * BM, n0 = nb * BN;
  PQKV P; P.A = h1; P.W = (nb < 32) ? Wq : (nb < 64 ? Wk : Wv); P.K = D_; P.ldb = HD_;
  f32x4 acc[BM / 32][BN / 32];
  gemm_tile<BM, BN, true>(P, m0, n0, smem, acc);
  const int lane = threadIdx.x & 63, wid = threadIdx.x >> 6, wm = wid >> 1, wn = wid & 1, fr = lane & 15, g = lane >> 4;
  const int mat = n0 / HD_, nn = n0 - mat * HD_;
  u16* o = out + (size_t)mat * S_ * HD_;
#pragma unroll
  for (int i = 0; i < BM / 32; ++i)
#pragma unroll
    for (int j = 0; j < BN / 32; ++j) {
      int tok = m0 + wm * (BM / 2) + i * 16 + fr, n = nn + wn * (BN / 2) + j * 16 + 4 * g;
      v2u v = {pack2(acc[i][j][0], acc[i][j][1]), pack2(acc[i][j][2], acc[i][j][3])};
      *(v2u*)(o + (size_t)tok * HD_ + n) = v;
    }
}

struct PU {
  const u16* V; const u16* Wv; const u16* Wo; int h; int K; int ldb;
  __device__ const u16* a_row(int m) const { return (m < S_ ? V + (size_t)m * HD_ : Wv + (size_t)(m - S_) * HD_) + h * DH_; }
  __device__ const u16* b_col(int n0, int sub) const { return Wo + (size_t)h * DH_ * D_ + n0 + sub * 64; }
};
__global__ void __launch_bounds__(256, 2) k_u(const u16* __restrict__ V, const u16* __restrict__ Wv, const u16* __restrict__ Wo, u16* __restrict__ U) {
  extern __shared__ __attribute__((aligned(16))) char smem[];
  constexpr int BM = 128, BN = 128;
  int b = blockIdx.x;
  const int nb = b % 5; b /= 5; const int mb = b % 21; const int h = b / 21;
  PU P; P.V = V; P.Wv = Wv; P.Wo = Wo; P.h = h; P.K = DH_; P.ldb = D_;
  const int m0 = mb * BM, n0 = nb * BN;
  f32x4 acc[BM / 32][BN / 32];
  gemm_tile<BM, BN, false>(P, m0, n0, smem, acc);
  const int lane = threadIdx.x & 63, wid = threadIdx.x >> 6, wm = wid >> 1, wn = wid & 1, fr = lane & 15, g = lane >> 4;
#pragma unroll
  for (int i = 0; i < BM / 32; ++i)
#pragma unroll
    for (int j = 0; j < BN / 32; ++j) {
      int row = m0 + wm * (BM / 2) + i * 16 + 4 * g, c = n0 + wn * (BN / 2) + j * 16 + fr;
      v2u v = {pack2(acc[i][j][0], acc[i][j][1]), pack2(acc[i][j][2], acc[i][j][3])};
      *(v2u*)(U + (((size_t)h * UCH_ + (row >> 3)) * D_ + c) * 8 + (row & 7)) = v;
    }
}

struct PF1 {
  const u16* A; const u16* w1; const u16* w3; int K; int ldb;
  __device__ const u16* a_row(int m) const { return A + (size_t)m * D_; }
  __device__ const u16* b_col(int n0, int sub) const { return (sub ? w3 : w1) + n0; }
};
__global__ void __launch_bounds__(256, 2) k_ffn1(const u16* __restrict__ h2, const u16* __restrict__ w1, const u16* __restrict__ w3, u16* __restrict__ G) {
  extern __shared__ __attribute__((aligned(16))) char smem[];
  constexpr int BM = 128, BN = 128;
  PF1 P; P.A = h2; P.w1 = w1; P.w3 = w3; P.K = D_; P.ldb = F_;
  const int nb = blockIdx.x % 24, mb = blockIdx.x / 24;
  const int m0 = mb * BM, f0 = nb * 64;
  f32x4 acc[BM / 32][BN / 32];
  gemm_tile<BM, BN, true>(P, m0, f0, smem, acc);
  const int lane = threadIdx.x & 63, wid = threadIdx.x >> 6, wm = wid >> 1, wn = wid & 1, fr = lane & 15, g = lane >> 4;
  f32x4* ex = (f32x4*)smem;
  if (wn == 1) {
#pragma unroll
    for (int i = 0; i < BM / 32; ++i)
#pragma unroll
      for (int j = 0; j < BN / 32; ++j) ex[((wm * (BM / 32) + i) * (BN / 32) + j) * 64 + lane] = acc[i][j];
  }
  __syncthreads();
  if (wn == 0) {
#pragma unroll
    for (int i = 0; i < BM / 32; ++i)
#pragma unroll
      for (int j = 0; j < BN / 32; ++j) {
        f32x4 up = ex[((wm * (BM / 32) + i) * (BN / 32) + j) * 64 + lane];
        float o[4];
#pragma unroll
        for (int r = 0; r < 4; ++r) {
          float gt = rbf(acc[i][j][r]);
          float sg = rbf(gt / (1.f + __expf(-gt)));
          o[r] = sg * rbf(up[r]);
        }
        int tok = m0 + wm * (BM / 2) + i * 16 + fr, f = f0 + j * 16 + 4 * g;
        v2u v = {pack2(o[0], o[1]), pack2(o[2], o[3])};
        *(v2u*)(G + (size_t)tok * F_ + f) = v;
      }
  }
}

struct PF2 {
  const u16* A; const u16* w2; int K; int ldb;
  __device__ const u16* a_row(int m) const { return A + (size_t)m * F_; }
  __device__ const u16* b_col(int n0, int sub) const { return w2 + n0 + sub * 64; }
};
__global__ void __launch_bounds__(256, 2) k_ffn2(const u16* __restrict__ G, const u16* __restrict__ w2, const u16* __restrict__ x1, const float* __restrict__ gb, u16* __restrict__ out) {
  extern __shared__ __attribute__((aligned(16))) char smem[];
  constexpr int BM = 64, BN = 64;
  PF2 P; P.A = G; P.w2 = w2; P.K = F_; P.ldb = D_;
  const int nb = blockIdx.x % 10, mb = blockIdx.x / 10;
  const int m0 = mb * BM, n0 = nb * BN;
  f32x4 acc[BM / 32][BN / 32];
  gemm_tile<BM, BN, true>(P, m0, n0, smem, acc);
  const int lane = threadIdx.x & 63, wid = threadIdx.x >> 6, wm = wid >> 1, wn = wid & 1, fr = lane & 15, g = lane >> 4;
#pragma unroll
  for (int i = 0; i < BM / 32; ++i)
#pragma unroll
    for (int j = 0; j < BN / 32; ++j) {
      int tok = m0 + wm * (BM / 2) + i * 16 + fr, c = n0 + wn * (BN / 2) + j * 16 + 4 * g;
      v2u xv = *(const v2u*)(x1 + (size_t)tok * D_ + c);
      float xo[4] = {bf2f((u16)(xv[0] & 0xFFFF)), bf2f((u16)(xv[0] >> 16)), bf2f((u16)(xv[1] & 0xFFFF)), bf2f((u16)(xv[1] >> 16))};
      float o[4];
#pragma unroll
      for (int r = 0; r < 4; ++r) o[r] = xo[r] + rbf(gb[c + r] * rbf(acc[i][j][r]));
      v2u v = {pack2(o[0], o[1]), pack2(o[2], o[3])};
      *(v2u*)(out + (size_t)tok * D_ + c) = v;
    }
}

struct ChainArgs {
  const u16* wt; const u16* U; const u16* Kx; u16* memb; u16* Pb; u16* km; u16* Um; unsigned* tmo;
};
constexpr int CH_LDS = 160 * 1024;
constexpr unsigned SPIN_CAP = 1u << 22;

__global__ void __launch_bounds__(256, 1) k_chain(ChainArgs a) {
  extern __shared__ __attribute__((aligned(16))) char smem[];
  const int b = blockIdx.x, tid = threadIdx.x, lane = tid & 63, w = __builtin_amdgcn_readfirstlane(tid >> 6);
  const int fr = lane & 15, g = lane >> 4;
  const __amdgpu_buffer_rsrc_t rm = __builtin_amdgcn_make_buffer_rsrc(a.memb, 0, 65 * 10240, 0x00020000);
  const __amdgpu_buffer_rsrc_t rp = __builtin_amdgcn_make_buffer_rsrc(a.Pb, 0, 64 * 20480, 0x00020000);
  if (b < H_) {
    const int h = b;
    char* lw = smem;
    char* lmem = smem + 144 * 1024;
    float* part = (float*)(smem + 154 * 1024);
    bf16x8 wr[84];
    const u16* wsrc = a.wt + ((size_t)(h * 4 + w) * 120 * 64 + lane) * 8;
#pragma unroll
    for (int f = 0; f < 80; ++f) wr[f] = *(const bf16x8*)(wsrc + (size_t)f * 512);
#pragma unroll
    for (int f = 0; f < 4; ++f) wr[80 + f] = *(const bf16x8*)(wsrc + (size_t)(116 + f) * 512);
    for (int f = 0; f < 36; ++f) *(bf16x8*)(lw + (w * 36 + f) * 1024 + lane * 16) = *(const bf16x8*)(wsrc + (size_t)(80 + f) * 512);
    const float scale = 0.07216878364870322f;
    for (int s = 0; s < NSEG_; ++s) {
      bf16x4 kx[2][3];
#pragma unroll
      for (int kt = 0; kt < 2; ++kt)
#pragma unroll
        for (int et = 0; et < 3; ++et)
          kx[kt][et] = *(const bf16x4*)(a.Kx + (size_t)(s * 32 + kt * 16 + fr) * HD_ + h * DH_ + (3 * w + et) * 16 + 4 * g);
      {
        v4u v[3];
        unsigned spins = 0;
        for (;;) {
          bool bad = false;
#pragma unroll
          for (int k = 0; k < 3; ++k) { int c = tid + 256 * k; c = c < 640 ? c : 0; v[k] = __builtin_amdgcn_raw_buffer_load_b128(rm, s * 10240 + c * 16, 0, 16); }
#pragma unroll
          for (int k = 0; k < 3; ++k) bad |= has_sent(v[k]);
          if (!__any(bad)) break;
          if (++spins > SPIN_CAP) { *a.tmo = 1; break; }
        }
#pragma unroll
        for (int k = 0; k < 3; ++k) { int c = tid + 256 * k; if (c < 640) *(v4u*)(lmem + c * 16) = v[k]; }
      }
      __syncthreads();
      f32x4 acc[6];
#pragma unroll
      for (int p = 0; p < 6; ++p) acc[p] = (f32x4){0.f, 0.f, 0.f, 0.f};
#pragma unroll
      for (int ks = 0; ks < 20; ++ks) {
        bf16x8 mf = *(const bf16x8*)(lmem + ((ks * 4 + g) * 8 + (fr & 7)) * 16);
#pragma unroll
        for (int p = 0; p < 4; ++p) acc[p] = __builtin_amdgcn_mfma_f32_16x16x32_bf16(wr[p * 20 + ks], mf, acc[p], 0, 0, 0);
        bf16x8 w4 = *(const bf16x8*)(lw + (w * 36 + ks) * 1024 + lane * 16);
        acc[4] = __builtin_amdgcn_mfma_f32_16x16x32_bf16(w4, mf, acc[4], 0, 0, 0);
        if (ks < 16) {
          bf16x8 w5 = *(const bf16x8*)(lw + (w * 36 + 20 + ks) * 1024 + lane * 16);
          acc[5] = __builtin_amdgcn_mfma_f32_16x16x32_bf16(w5, mf, acc[5], 0, 0, 0);
        } else {
          acc[5] = __builtin_amdgcn_mfma_f32_16x16x32_bf16(wr[80 + ks - 16], mf, acc[5], 0, 0, 0);
        }
      }
      bf16x4 qf[3], kf[3];
#pragma unroll
      for (int et = 0; et < 3; ++et) {
        v2u a0 = {pack2(acc[et][0], acc[et][1]), pack2(acc[et][2], acc[et][3])};
        v2u a1 = {pack2(acc[3 + et][0], acc[3 + et][1]), pack2(acc[3 + et][2], acc[3 + et][3])};
        qf[et] = __builtin_bit_cast(bf16x4, a0); kf[et] = __builtin_bit_cast(bf16x4, a1);
        if (fr < 8) *(v2u*)(a.km + ((size_t)s * 8 + fr) * HD_ + h * DH_ + (3 * w + et) * 16 + 4 * g) = a1;
      }
      f32x4 st[3];
#pragma unroll
      for (int kt = 0; kt < 3; ++kt) st[kt] = (f32x4){0.f, 0.f, 0.f, 0.f};
#pragma unroll
      for (int et = 0; et < 3; ++et) {
        st[0] = __builtin_amdgcn_mfma_f32_16x16x16bf16_1k(kf[et], qf[et], st[0], 0, 0, 0);
        st[1] = __builtin_amdgcn_mfma_f32_16x16x16bf16_1k(kx[0][et], qf[et], st[1], 0, 0, 0);
        st[2] = __builtin_amdgcn_mfma_f32_16x16x16bf16_1k(kx[1][et], qf[et], st[2], 0, 0, 0);
      }
      if (fr < 8) {
        if (g < 2) *(f32x4*)(part + (w * 8 + fr) * 40 + 4 * g) = st[0];
        *(f32x4*)(part + (w * 8 + fr) * 40 + 8 + 4 * g) = st[1];
        *(f32x4*)(part + (w * 8 + fr) * 40 + 24 + 4 * g) = st[2];
      }
      __syncthreads();
      if (w == 0) {
        const int i = lane & 7, jc = lane >> 3;
        float sc[8];
        float mx = -3.0e38f;
        if (jc < 5) {
#pragma unroll
          for (int e = 0; e < 8; ++e) sc[e] = 0.f;
#pragma unroll
          for (int ww = 0; ww < 4; ++ww) {
            f32x4 p0 = *(const f32x4*)(part + (ww * 8 + i) * 40 + jc * 8), p1 = *(const f32x4*)(part + (ww * 8 + i) * 40 + jc * 8 + 4);
#pragma unroll
            for (int e = 0; e < 4; ++e) { sc[e] += p0[e]; sc[4 + e] += p1[e]; }
          }
#pragma unroll
          for (int e = 0; e < 8; ++e) { sc[e] = rbf(rbf(sc[e]) * scale); mx = fmaxf(mx, sc[e]); }
        }
        mx = fmaxf(mx, __shfl_xor(mx, 8)); mx = fmaxf(mx, __shfl_xor(mx, 16)); mx = fmaxf(mx, __shfl_xor(mx, 32));
        float sum = 0.f;
        if (jc < 5) {
#pragma unroll
          for (int e = 0; e < 8; ++e) { sc[e] = __expf(sc[e] - mx); sum += sc[e]; }
        }
        sum += __shfl_xor(sum, 8); sum += __shfl_xor(sum, 16); sum += __shfl_xor(sum, 32);
        if (jc < 5) {
          const float inv = 1.f / sum;
          v4u o = {canon2(pack2(sc[0] * inv, sc[1] * inv)), canon2(pack2(sc[2] * inv, sc[3] * inv)), canon2(pack2(sc[4] * inv, sc[5] * inv)), canon2(pack2(sc[6] * inv, sc[7] * inv))};
          __builtin_amdgcn_raw_buffer_store_b128(o, rp, s * 20480 + h * 640 + lane * 16, 0, 16);
        }
      }
    }
  } else if (b < H_ + NU_) {
    const int u = b - H_;
    char* lmem = smem;
    char* lub = smem + 10240;
    float* red = (float*)(smem + 30720);
    bf16x8 br[80];
#pragma unroll
    for (int nt = 0; nt < 4; ++nt)
#pragma unroll
      for (int ks = 0; ks < 20; ++ks) {
        int hh = (4 * w + nt) * 2 + (fr >> 3), c = 8 * u + (fr & 7);
        br[nt * 20 + ks] = *(const bf16x8*)(a.U + (((size_t)hh * UCH_ + 256 + ks * 4 + g) * D_ + c) * 8);
      }
    for (int s = 0; s < NSEG_; ++s) {
#pragma unroll
      for (int k = 0; k < 4; ++k) {
        int c = tid + 256 * k;
        int hh = c >> 5, jc = (c >> 3) & 3, cc = c & 7;
        v4u v = *(const v4u*)(a.U + (((size_t)hh * UCH_ + s * 4 + jc) * D_ + 8 * u + cc) * 8);
        *(v4u*)(lub + ((hh * 5 + 1 + jc) * 8 + cc) * 16) = v;
      }
      {
        v4u v[3];
        unsigned spins = 0;
        for (;;) {
          bool bad = false;
#pragma unroll
          for (int k = 0; k < 3; ++k) { int c = tid + 256 * k; c = c < 640 ? c : 0; v[k] = __builtin_amdgcn_raw_buffer_load_b128(rm, s * 10240 + c * 16, 0, 16); }
#pragma unroll
          for (int k = 0; k < 3; ++k) bad |= has_sent(v[k]);
          if (!__any(bad)) break;
          if (++spins > SPIN_CAP) { *a.tmo = 1; break; }
        }
#pragma unroll
        for (int k = 0; k < 3; ++k) { int c = tid + 256 * k; if (c < 640) *(v4u*)(lmem + c * 16) = v[k]; }
      }
      __syncthreads();
      {
        f32x4 acc[4];
#pragma unroll
        for (int nt = 0; nt < 4; ++nt) acc[nt] = (f32x4){0.f, 0.f, 0.f, 0.f};
#pragma unroll
        for (int ks = 0; ks < 20; ++ks) {
          bf16x8 mf = *(const bf16x8*)(lmem + ((ks * 4 + g) * 8 + (fr & 7)) * 16);
#pragma unroll
          for (int nt = 0; nt < 4; ++nt) acc[nt] = __builtin_amdgcn_mfma_f32_16x16x32_bf16(mf, br[nt * 20 + ks], acc[nt], 0, 0, 0);
        }
        if (g < 2) {
#pragma unroll
          for (int nt = 0; nt < 4; ++nt) {
            int hh = (4 * w + nt) * 2 + (fr >> 3), cc = fr & 7;
            v2u v = {pack2(acc[nt][0], acc[nt][1]), pack2(acc[nt][2], acc[nt][3])};
            *(v2u*)(lub + ((hh * 5) * 8 + cc) * 16 + g * 8) = v;
            *(v2u*)(a.Um + (((size_t)s * H_ + hh) * D_ + 8 * u + cc) * 8 + g * 4) = v;
          }
        }
      }
      __syncthreads();
      bf16x8 ub[10], pf[10];
#pragma unroll
      for (int k = 0; k < 10; ++k) ub[k] = *(const bf16x8*)(lub + (((w * 10 + k) * 4 + g) * 8 + (fr & 7)) * 16);
      {
        unsigned spins = 0;
        for (;;) {
          bool bad = false;
          v4u v[10];
#pragma unroll
          for (int k = 0; k < 10; ++k) v[k] = __builtin_amdgcn_raw_buffer_load_b128(rp, s * 20480 + (((w * 10 + k) * 4 + g) * 8 + (fr & 7)) * 16, 0, 16);
#pragma unroll
          for (int k = 0; k < 10; ++k) { bad |= has_sent(v[k]); pf[k] = __builtin_bit_cast(bf16x8, v[k]); }
          if (!__any(bad)) break;
          if (++spins > SPIN_CAP) { *a.tmo = 1; break; }
        }
      }
      f32x4 acc = {0.f, 0.f, 0.f, 0.f};
#pragma unroll
      for (int k = 0; k < 10; ++k) acc = __builtin_amdgcn_mfma_f32_16x16x32_bf16(pf[k], ub[k], acc, 0, 0, 0);
      if (fr < 8 && g < 2) {
#pragma unroll
        for (int r = 0; r < 4; ++r) red[(w * 8 + 4 * g + r) * 8 + fr] = acc[r];
      }
      __syncthreads();
      if (tid < 8) {
        float o[8];
#pragma unroll
        for (int c = 0; c < 8; ++c) o[c] = red[(0 * 8 + tid) * 8 + c] + red[(1 * 8 + tid) * 8 + c] + red[(2 * 8 + tid) * 8 + c] + red[(3 * 8 + tid) * 8 + c];
        v4u v = {canon2(pack2(o[0], o[1])), canon2(pack2(o[2], o[3])), canon2(pack2(o[4], o[5])), canon2(pack2(o[6], o[7]))};
        __builtin_amdgcn_raw_buffer_store_b128(v, rm, (s + 1) * 10240 + u * 128 + tid * 16, 0, 16);
      }
    }
  }
}

__global__ void __launch_bounds__(256) k_xp(const u16* __restrict__ Q, const u16* __restrict__ Kx, const u16* __restrict__ km, u16* __restrict__ Pall) {
  const int lane = threadIdx.x & 63, wv = blockIdx.x * 4 + (threadIdx.x >> 6);
  const int s = wv >> 5, h = wv & 31, fr = lane & 15, g = lane >> 4;
  f32x4 acc[3][2];
#pragma unroll
  for (int kt = 0; kt < 3; ++kt) { acc[kt][0] = (f32x4){0.f, 0.f, 0.f, 0.f}; acc[kt][1] = (f32x4){0.f, 0.f, 0.f, 0.f}; }
#pragma unroll
  for (int ks = 0; ks < 6; ++ks) {
    const int eo = h * DH_ + ks * 32 + 8 * g;
    bf16x8 kf[3], qf[2];
    kf[0] = *(const bf16x8*)(km + ((size_t)s * 8 + (fr & 7)) * HD_ + eo);
    kf[1] = *(const bf16x8*)(Kx + (size_t)(s * 32 + fr) * HD_ + eo);
    kf[2] = *(const bf16x8*)(Kx + (size_t)(s * 32 + 16 + fr) * HD_ + eo);
    qf[0] = *(const bf16x8*)(Q + (size_t)(s * 32 + fr) * HD_ + eo);
    qf[1] = *(const bf16x8*)(Q + (size_t)(s * 32 + 16 + fr) * HD_ + eo);
#pragma unroll
    for (int kt = 0; kt < 3; ++kt)
#pragma unroll
      for (int tt = 0; tt < 2; ++tt) acc[kt][tt] = __builtin_amdgcn_mfma_f32_16x16x32_bf16(kf[kt], qf[tt], acc[kt][tt], 0, 0, 0);
  }
  const float scale = 0.07216878364870322f;
#pragma unroll
  for (int tt = 0; tt < 2; ++tt) {
    const int t = tt * 16 + fr;
    float sc[12]; bool ok[12];
    float mx = -3.0e38f;
#pragma unroll
    for (int kt = 0; kt < 3; ++kt)
#pragma unroll
      for (int r = 0; r < 4; ++r) {
        int jl = 4 * g + r;
        bool v = (kt == 0) ? (jl < 8) : ((kt - 1) * 16 + jl <= t);
        float x = rbf(rbf(acc[kt][tt][r]) * scale);
        sc[kt * 4 + r] = x; ok[kt * 4 + r] = v;
        if (v) mx = fmaxf(mx, x);
      }
    mx = fmaxf(mx, __shfl_xor(mx, 16)); mx = fmaxf(mx, __shfl_xor(mx, 32));
    float sum = 0.f;
#pragma unroll
    for (int e = 0; e < 12; ++e) { sc[e] = ok[e] ? __expf(sc[e] - mx) : 0.f; sum += sc[e]; }
    sum += __shfl_xor(sum, 16); sum += __shfl_xor(sum, 32);
    const float inv = 1.f / sum;
    u16* dst = Pall + (size_t)(s * 32 + t) * 1280 + h * 40;
    if (g < 2) { v2u v = {pack2(sc[0] * inv, sc[1] * inv), pack2(sc[2] * inv, sc[3] * inv)}; *(v2u*)(dst + 4 * g) = v; }
    { v2u v = {pack2(sc[4] * inv, sc[5] * inv), pack2(sc[6] * inv, sc[7] * inv)}; *(v2u*)(dst + 8 + 4 * g) = v; }
    { v2u v = {pack2(sc[8] * inv, sc[9] * inv), pack2(sc[10] * inv, sc[11] * inv)}; *(v2u*)(dst + 24 + 4 * g) = v; }
  }
}

__global__ void __launch_bounds__(256) k_xo(const u16* __restrict__ Pall, const u16* __restrict__ U, const u16* __restrict__ Um, const u16* __restrict__ x, const float* __restrict__ ga, u16* __restrict__ x1) {
  const int lane = threadIdx.x & 63, w = threadIdx.x >> 6, fr = lane & 15, g = lane >> 4;
  const int s = blockIdx.x / 5, cg = blockIdx.x % 5;
  const int c0 = cg * 128 + w * 32;
  f32x4 acc[2][2];
#pragma unroll
  for (int i = 0; i < 2; ++i) { acc[i][0] = (f32x4){0.f, 0.f, 0.f, 0.f}; acc[i][1] = (f32x4){0.f, 0.f, 0.f, 0.f}; }
#pragma unroll 4
  for (int ks = 0; ks < 40; ++ks) {
    const int kc = ks * 4 + g, h = kc / 5, jc = kc - h * 5;
    const u16* ub = (jc == 0) ? Um + (((size_t)s * H_ + h) * D_) * 8 : U + (((size_t)h * UCH_ + s * 4 + jc - 1) * D_) * 8;
    bf16x8 uf[2], pf[2];
    uf[0] = *(const bf16x8*)(ub + (size_t)(c0 + fr) * 8);
    uf[1] = *(const bf16x8*)(ub + (size_t)(c0 + 16 + fr) * 8);
    pf[0] = *(const bf16x8*)(Pall + (size_t)(s * 32 + fr) * 1280 + kc * 8);
    pf[1] = *(const bf16x8*)(Pall + (size_t)(s * 32 + 16 + fr) * 1280 + kc * 8);
#pragma unroll
    for (int ct = 0; ct < 2; ++ct)
#pragma unroll
      for (int tt = 0; tt < 2; ++tt) acc[ct][tt] = __builtin_amdgcn_mfma_f32_16x16x32_bf16(uf[ct], pf[tt], acc[ct][tt], 0, 0, 0);
  }
#pragma unroll
  for (int ct = 0; ct < 2; ++ct)
#pragma unroll
    for (int tt = 0; tt < 2; ++tt) {
      const int tok = s * 32 + tt * 16 + fr, c = c0 + ct * 16 + 4 * g;
      v2u xv = *(const v2u*)(x + (size_t)tok * D_ + c);
      float xo[4] = {bf2f((u16)(xv[0] & 0xFFFF)), bf2f((u16)(xv[0] >> 16)), bf2f((u16)(xv[1] & 0xFFFF)), bf2f((u16)(xv[1] >> 16))};
      float o[4];
#pragma unroll
      for (int r = 0; r < 4; ++r) o[r] = xo[r] + rbf(ga[c + r] * rbf(acc[ct][tt][r]));
      v2u v = {pack2(o[0], o[1]), pack2(o[2], o[3])};
      *(v2u*)(x1 + (size_t)tok * D_ + c) = v;
    }
}

extern "C" void kernel_launch(void* const* d_in, const int* in_sizes, int n_in,
                              void* d_out, int out_size, void* d_ws, size_t ws_size,
                              hipStream_t stream) {
  const u16* x = (const u16*)d_in[0];
  const u16* mem0 = (const u16*)d_in[1];
  const u16* Wq = (const u16*)d_in[2];
  const u16* Wk = (const u16*)d_in[3];
  const u16* Wv = (const u16*)d_in[4];
  const u16* Wo = (const u16*)d_in[5];
  const u16* w1 = (const u16*)d_in[8];
  const u16* w3 = (const u16*)d_in[9];
  const u16* w2 = (const u16*)d_in[10];
  const u16* c = (const u16*)d_in[11];
  const u16* wmod = (const u16*)d_in[12];
  char* ws = (char*)d_ws;
  u16* out = (u16*)d_out;
  float* mod = (float*)(ws + WS_MOD);
  u16* memb = (u16*)(ws + WS_MEMB); u16* Pb = (u16*)(ws + WS_PB);
  u16* h1 = (u16*)(ws + WS_H1); u16* Q = (u16*)(ws + WS_Q); u16* K = (u16*)(ws + WS_K); u16* V = (u16*)(ws + WS_V);
  u16* U = (u16*)(ws + WS_U); u16* wt = (u16*)(ws + WS_WT); u16* km = (u16*)(ws + WS_KM); u16* Um = (u16*)(ws + WS_UM);
  u16* Pall = (u16*)(ws + WS_PALL); u16* x1 = (u16*)(ws + WS_X1); u16* h2 = (u16*)(ws + WS_H2); u16* G = (u16*)(ws + WS_G);

  hipFuncSetAttribute((const void*)k_qkv, hipFuncAttributeMaxDynamicSharedMemorySize, 2 * (128 + 192) * 128);
  hipFuncSetAttribute((const void*)k_u, hipFuncAttributeMaxDynamicSharedMemorySize, 2 * (128 + 128) * 128);
  hipFuncSetAttribute((const void*)k_ffn1, hipFuncAttributeMaxDynamicSharedMemorySize, 2 * (128 + 128) * 128);
  hipFuncSetAttribute((const void*)k_chain, hipFuncAttributeMaxDynamicSharedMemorySize, CH_LDS);

  hipMemsetAsync(ws, 0, 256, stream);
  hipMemsetAsync(ws + WS_MEMB, 0xFF, WS_SENT_END - WS_MEMB, stream);
  k_mod<<<60, 256, 0, stream>>>(c, wmod, mod);
  k_prep<<<(2 * D_ * HD_ / 8) / 256, 256, 0, stream>>>(mem0, Wq, Wk, memb, wt);
  k_ln<<<S_ / 4, 256, 0, stream>>>(x, mod + 0 * D_, mod + 1 * D_, h1);
  k_qkv<<<16 * 96, 256, 2 * (128 + 192) * 128, stream>>>(h1, Wq, Wk, Wv, Q);
  k_u<<<32 * 21 * 5, 256, 2 * (128 + 128) * 128, stream>>>(V, Wv, Wo, U);
  ChainArgs ca; ca.wt = wt; ca.U = U; ca.Kx = K; ca.memb = memb; ca.Pb = Pb; ca.km = km; ca.Um = Um; ca.tmo = (unsigned*)(ws + WS_TMO);
  k_chain<<<H_ + NU_, 256, CH_LDS, stream>>>(ca);
  k_xp<<<NSEG_ * H_ / 4, 256, 0, stream>>>(Q, K, km, Pall);
  k_xo<<<NSEG_ * 5, 256, 0, stream>>>(Pall, U, Um, x, mod + 2 * D_, x1);
  k_ln<<<S_ / 4, 256, 0, stream>>>(x1, mod + 3 * D_, mod + 4 * D_, h2);
  k_ffn1<<<16 * 24, 256, 2 * (128 + 128) * 128, stream>>>(h2, w1, w3, G);
  k_ffn2<<<32 * 10, 256, 2 * (64 + 64) * 128, stream>>>(G, w2, x1, mod + 5 * D_, out);
}
```

```cpp
#include <hip/hip_runtime.h>
#include <hip/hip_bf16.h>
#include <stdint.h>

constexpr int S_ = 2048, D_ = 640, H_ = 32, DH_ = 192, NSEG_ = 64, F_ = 1536;
constexpr int HD_ = H_ * DH_;
constexpr int NU_ = 80;
constexpr int UROWS_ = S_ + D_;
constexpr int UCH_ = UROWS_ / 8;
constexpr int NCHAIN_ = H_ + NU_;
constexpr int NBLK_ = 256;
constexpr int NWRK_ = NBLK_ - NCHAIN_;
constexpr int NMT_ = 16;

typedef short bf16x8 __attribute__((ext_vector_type(8)));
typedef short bf16x4 __attribute__((ext_vector_type(4)));
typedef float f32x4 __attribute__((ext_vector_type(4)));
typedef unsigned v4u __attribute__((ext_vector_type(4)));
typedef unsigned v2u __attribute__((ext_vector_type(2)));
typedef unsigned short u16;
typedef short v4i16_t __attribute__((ext_vector_type(4)));

__device__ __forceinline__ float bf2f(u16 x) { return __uint_as_float(((unsigned)x) << 16); }
__device__ __forceinline__ float bflo(unsigned x) { return __uint_as_float(x << 16); }
__device__ __forceinline__ float bfhi(unsigned x) { return __uint_as_float(x & 0xFFFF0000u); }
__device__ __forceinline__ u16 f2bf(float f) { return __builtin_bit_cast(u16, (__bf16)f); }
__device__ __forceinline__ float rbf(float f) { return bf2f(f2bf(f)); }
typedef __bf16 bf2_t __attribute__((ext_vector_type(2)));
typedef float f2_t __attribute__((ext_vector_type(2)));
__device__ __forceinline__ unsigned pack2(float a, float b) { f2_t f = {a, b}; bf2_t r = __builtin_convertvector(f, bf2_t); return __builtin_bit_cast(unsigned, r); }
__device__ __forceinline__ unsigned canon2(unsigned x) { return x == 0xFFFFFFFFu ? 0xFFFFFFFEu : x; }
__device__ __forceinline__ unsigned umax4(unsigned m, v4u v) {
  m = max(max(m, v[0]), v[1]);
  return max(max(m, v[2]), v[3]);
}

constexpr size_t al256(size_t x) { return (x + 255) & ~(size_t)255; }
constexpr size_t WS_CTL  = 0;
constexpr size_t CTL_BYTES = 32768;
constexpr size_t WS_MEMB = CTL_BYTES;
constexpr size_t WS_PB   = WS_MEMB + (size_t)65 * 10240;
constexpr size_t WS_SENT_END = WS_PB + (size_t)64 * 20480;
constexpr size_t WS_MOD  = al256(WS_SENT_END);
constexpr size_t WS_H1   = al256(WS_MOD + 3840 * 4);
constexpr size_t WS_Q    = al256(WS_H1 + (size_t)S_ * D_ * 2);
constexpr size_t WS_K    = WS_Q + (size_t)S_ * HD_ * 2;
constexpr size_t WS_U    = WS_K + (size_t)S_ * HD_ * 2;
constexpr size_t WS_WT   = WS_U + (size_t)H_ * UCH_ * D_ * 8 * 2;
constexpr size_t WS_KM   = WS_WT + (size_t)2 * D_ * HD_ * 2;
constexpr size_t WS_UM   = WS_KM + (size_t)NSEG_ * 8 * HD_ * 2;
constexpr size_t WS_X1   = WS_UM + (size_t)NSEG_ * H_ * D_ * 8 * 2;
constexpr size_t WS_H2   = WS_X1 + (size_t)S_ * D_ * 2;
constexpr size_t WS_G    = WS_H2 + (size_t)S_ * D_ * 2;
constexpr size_t WS_XP   = WS_G + (size_t)S_ * F_ * 2;
constexpr size_t WS_END  = WS_XP + (size_t)NSEG_ * 4 * 32 * D_ * 4;
constexpr int C_TMO = 0, C_K = 1, C_X = 49, C_F = 65, C_S = 81, C_T = 145, C_QUEUE = 210, C_XS = 256, C_FS = 320;
__device__ __forceinline__ unsigned* ctl(char* ws, int idx) { return (unsigned*)(ws + WS_CTL) + idx * 16; }

constexpr unsigned SPIN_CAP = 1u << 22;

__device__ __forceinline__ void spin_ge(unsigned* c, unsigned target, unsigned* tmo) {
  unsigned spins = 0;
  while (__hip_atomic_load(c, __ATOMIC_RELAXED, __HIP_MEMORY_SCOPE_AGENT) < target) {
    __builtin_amdgcn_s_sleep(2);
    if (++spins > SPIN_CAP) { __hip_atomic_store(tmo, 1u, __ATOMIC_RELAXED, __HIP_MEMORY_SCOPE_AGENT); break; }
  }
}
__device__ __forceinline__ void acquire_all() {
  __builtin_amdgcn_fence(__ATOMIC_ACQUIRE, "agent");
  asm volatile("s_waitcnt vmcnt(0)" ::: "memory");
}
__device__ __forceinline__ void publish(unsigned* c) {
  asm volatile("s_waitcnt vmcnt(0)" ::: "memory");
  __syncthreads();
  if (threadIdx.x == 64) __hip_atomic_fetch_add(c, 1u, __ATOMIC_RELAXED, __HIP_MEMORY_SCOPE_AGENT);
  __syncthreads();
}

__device__ __forceinline__ v4u pair16(v2u A, v2u B) {
  auto r0 = __builtin_amdgcn_permlane16_swap(A[0], B[0], false, false);
  auto r1 = __builtin_amdgcn_permlane16_swap(A[1], B[1], false, false);
  unsigned a0 = r0[0], b0 = r0[1], a1 = r1[0], b1 = r1[1];
  return (v4u){a0, a1, b0, b1};
}
__device__ __forceinline__ int otid() { int t = threadIdx.x; asm volatile("" : "+v"(t)); return t; }
__device__ __forceinline__ v2u pk4(f32x4 v) { return (v2u){pack2(v[0], v[1]), pack2(v[2], v[3])}; }

__global__ void __launch_bounds__(256) k_ln(const u16* __restrict__ x, const float* __restrict__ shift, const float* __restrict__ scale, u16* __restrict__ y) {
  const int lane = threadIdx.x & 63, w = threadIdx.x >> 6;
  const int row = blockIdx.x * 4 + w;
  const unsigned* xr = (const unsigned*)(x + (size_t)row * D_);
  float v[10];
  float s = 0.f;
#pragma unroll
  for (int i = 0; i < 5; ++i) { unsigned u = xr[lane + 64 * i]; v[2 * i] = bflo(u); v[2 * i + 1] = bfhi(u); s += v[2 * i] + v[2 * i + 1]; }
#pragma unroll
  for (int m = 32; m >= 1; m >>= 1) s += __shfl_xor(s, m);
  const float mean = s * (1.f / D_);
  float q = 0.f;
#pragma unroll
  for (int i = 0; i < 10; ++i) { float d = v[i] - mean; q += d * d; }
#pragma unroll
  for (int m = 32; m >= 1; m >>= 1) q += __shfl_xor(q, m);
  const float mb = rbf(mean), vb = rbf(q * (1.f / D_));
  const float den = rbf(sqrtf(rbf(vb + 1e-5f)));
  unsigned* yr = (unsigned*)(y + (size_t)row * D_);
#pragma unroll
  for (int i = 0; i < 5; ++i) {
    int c = 2 * (lane + 64 * i);
    float o[2];
#pragma unroll
    for (int e = 0; e < 2; ++e) {
      float t = rbf(rbf(v[2 * i + e] - mb) / den);
      t = rbf(t * rbf(1.f + scale[c + e]));
      o[e] = t + shift[c + e];
    }
    yr[lane + 64 * i] = pack2(o[0], o[1]);
  }
}

__device__ __forceinline__ int fswz(int r) { return (((r >> 1) & 1) << 1) | (((r >> 3) & 1) << 2); }

__device__ __forceinline__ bf16x8 tr_frag(const char* sub_base, int krow0, int c0, int lane) {
  const int g = lane >> 4, q = (lane & 15) >> 2, pp = lane & 3;
  v4i16_t lo, hi;
  { int r = krow0 + 8 * g + q; lo = __builtin_amdgcn_ds_read_tr16_b64_v4i16((__attribute__((address_space(3))) v4i16_t*)(sub_base + r * 128 + (((c0 + (pp >> 1)) ^ fswz(r)) * 16) + 8 * (pp & 1))); }
  { int r = krow0 + 8 * g + 4 + q; hi = __builtin_amdgcn_ds_read_tr16_b64_v4i16((__attribute__((address_space(3))) v4i16_t*)(sub_base + r * 128 + (((c0 + (pp >> 1)) ^ fswz(r)) * 16) + 8 * (pp & 1))); }
  return (bf16x8){lo[0], lo[1], lo[2], lo[3], hi[0], hi[1], hi[2], hi[3]};
}

template <int BM, int BN, bool SWAP, class PT>
__device__ __forceinline__ void gemm_tile(const PT& P, int m0, int n0, char* smem, f32x4 (&acc)[BM / 32][BN / 32]) {
  constexpr int ABYTES = BM * 128, BBYTES = BN * 128, STG = ABYTES + BBYTES;
  constexpr int NA = BM * 8 / 256, NB = BN * 8 / 256;
  const int tid = threadIdx.x, lane = tid & 63, wid = tid >> 6, wm = wid >> 1, wn = wid & 1;
  const int K = P.K;
  const u16* asrc[NA];
  const u16* bsrc[NB];
#pragma unroll
  for (int it = 0; it < NA; ++it) { int p = it * 256 + tid; int row = p >> 3, slot = p & 7; asrc[it] = P.a_row(m0 + row) + ((slot ^ (row & 7)) * 8); }
#pragma unroll
  for (int it = 0; it < NB; ++it) { int p = it * 256 + tid; int sub = p >> 9, r = (p >> 3) & 63, slot = p & 7; bsrc[it] = P.b_col(n0, sub) + (size_t)r * P.ldb + ((slot ^ fswz(r)) * 8); }
  auto stage = [&](int kt, int buf) {
    char* base = smem + buf * STG;
#pragma unroll
    for (int it = 0; it < NA; ++it)
      __builtin_amdgcn_global_load_lds((const unsigned*)(asrc[it] + kt * 64), (unsigned*)(base + (it * 256 + tid) * 16), 16, 0, 0);
#pragma unroll
    for (int it = 0; it < NB; ++it)
      __builtin_amdgcn_global_load_lds((const unsigned*)(bsrc[it] + (size_t)kt * 64 * P.ldb), (unsigned*)(base + ABYTES + (it * 256 + tid) * 16), 16, 0, 0);
  };
#pragma unroll
  for (int i = 0; i < BM / 32; ++i)
#pragma unroll
    for (int j = 0; j < BN / 32; ++j) acc[i][j] = (f32x4){0.f, 0.f, 0.f, 0.f};
  const int nk = K / 64;
  const int fr = lane & 15, g = lane >> 4;
  stage(0, 0);
  __syncthreads();
  for (int kt = 0; kt < nk; ++kt) {
    const int cur = kt & 1;
    if (kt + 1 < nk) stage(kt + 1, cur ^ 1);
    char* abase = smem + cur * STG;
    char* bbase = abase + ABYTES;
#pragma unroll
    for (int kk = 0; kk < 2; ++kk) {
      bf16x8 af[BM / 32], bf[BN / 32];
#pragma unroll
      for (int i = 0; i < BM / 32; ++i) {
        int row = wm * (BM / 2) + i * 16 + fr;
        af[i] = *(const bf16x8*)(abase + row * 128 + (((kk * 4 + g) ^ (row & 7)) * 16));
      }
#pragma unroll
      for (int j = 0; j < BN / 32; ++j) {
        int col = wn * (BN / 2) + j * 16;
        bf[j] = tr_frag(bbase + (col >> 6) * 8192, kk * 32, (col & 63) >> 3, lane);
      }
#pragma unroll
      for (int i = 0; i < BM / 32; ++i)
#pragma unroll
        for (int j = 0; j < BN / 32; ++j)
          acc[i][j] = SWAP ? __builtin_amdgcn_mfma_f32_16x16x32_bf16(bf[j], af[i], acc[i][j], 0, 0, 0)
                           : __builtin_amdgcn_mfma_f32_16x16x32_bf16(af[i], bf[j], acc[i][j], 0, 0, 0);
    }
    __syncthreads();
  }
}

__device__ __forceinline__ void glds16(const void* gsrc, unsigned lds_dst) {
  unsigned keep;
  asm volatile("s_mov_b32 %0, m0\n\ts_mov_b32 m0, %2\n\ts_nop 0\n\tglobal_load_lds_dwordx4 %1, off\n\ts_mov_b32 m0, %0"
               : "=&s"(keep) : "v"(gsrc), "s"(lds_dst) : "memory");
}
#define WAITV(n) asm volatile("s_waitcnt vmcnt(%0)" :: "n"(n) : "memory")
#define RAWBAR() do { asm volatile("s_waitcnt lgkmcnt(0)" ::: "memory"); __builtin_amdgcn_s_barrier(); asm volatile("" ::: "memory"); } while (0)
template <int BM, int BN, int NK, int NBUF, bool SWAP, class PT>
__device__ __forceinline__ void gemm_tile_dma(const PT& P, int m0, int n0, char* smem, f32x4 (&acc)[BM / 32][BN / 32]) {
  constexpr int ABYTES = BM * 128, BBYTES = BN * 128, STG = ABYTES + BBYTES;
  constexpr int NA = BM * 8 / 256, NB = BN * 8 / 256, NOP = NA + NB;
  const int tid = otid(), lane = tid & 63, wid = tid >> 6, wm = wid >> 1, wn = wid & 1;
  const u16* asrc[NA];
  const u16* bsrc[NB];
#pragma unroll
  for (int it = 0; it < NA; ++it) { int p = it * 256 + tid; int row = p >> 3, slot = p & 7; asrc[it] = P.a_row(m0 + row) + ((slot ^ (row & 7)) * 8); }
#pragma unroll
  for (int it = 0; it < NB; ++it) { int p = it * 256 + tid; int sub = p >> 9, r = (p >> 3) & 63, slot = p & 7; bsrc[it] = P.b_col(n0, sub) + (size_t)r * P.ldb + ((slot ^ fswz(r)) * 8); }
  const unsigned ldsw = __builtin_amdgcn_readfirstlane((unsigned)(size_t)smem + (tid >> 6) * 1024);
  auto stage_op = [&](int kt, int op) {
    const unsigned base = ldsw + (kt % NBUF) * STG;
    if (op < NA) glds16(asrc[op] + kt * 64, base + op * 4096);
    else glds16(bsrc[op - NA] + (size_t)kt * 64 * P.ldb, base + ABYTES + (op - NA) * 4096);
  };
#pragma unroll
  for (int i = 0; i < BM / 32; ++i)
#pragma unroll
    for (int j = 0; j < BN / 32; ++j) acc[i][j] = (f32x4){0.f, 0.f, 0.f, 0.f};
  const int fr = lane & 15, g = lane >> 4;
#pragma unroll
  for (int d = 0; d < NBUF - 1; ++d)
#pragma unroll
    for (int op = 0; op < NOP; ++op) stage_op(d, op);
  constexpr int NI = BM / 32, NJ = BN / 32, NM = 2 * NI * NJ;
  constexpr int EVERY = NM / NOP;
#pragma unroll
  for (int kt = 0; kt < NK; ++kt) {
    const int after = (kt + NBUF - 2 < NK - 1 ? NBUF - 2 : NK - 1 - kt);
    if (after == 0) WAITV(0); else if (after == 1) WAITV(NOP); else if (after == 2) WAITV(2 * NOP); else if (after == 3) WAITV(3 * NOP); else if (after == 4) WAITV(4 * NOP); else if (after == 5) WAITV(5 * NOP); else WAITV(6 * NOP);
    RAWBAR();
    char* base = smem + (kt % NBUF) * STG;
    char* bbase = base + ABYTES;
    bf16x8 af[2][NI], bf[2][NJ];
#pragma unroll
    for (int kk = 0; kk < 2; ++kk) {
#pragma unroll
      for (int j = 0; j < NJ; ++j) {
        int col = wn * (BN / 2) + j * 16;
        bf[kk][j] = tr_frag(bbase + (col >> 6) * 8192, kk * 32, (col & 63) >> 3, lane);
      }
#pragma unroll
      for (int i = 0; i < NI; ++i) {
        int row = wm * (BM / 2) + i * 16 + fr;
        af[kk][i] = *(const bf16x8*)(base + row * 128 + (((kk * 4 + g) ^ (row & 7)) * 16));
      }
    }
    __builtin_amdgcn_sched_barrier(0);
    if (kt + NBUF - 1 < NK) stage_op(kt + NBUF - 1, 0);
#pragma unroll
    for (int m = 0; m < NM; ++m) {
      const int kk = m / (NI * NJ), i = (m / NJ) % NI, j = m % NJ;
      acc[i][j] = SWAP ? __builtin_amdgcn_mfma_f32_16x16x32_bf16(bf[kk][j], af[kk][i], acc[i][j], 0, 0, 0)
                       : __builtin_amdgcn_mfma_f32_16x16x32_bf16(af[kk][i], bf[kk][j], acc[i][j], 0, 0, 0);
      if ((m + 1) % EVERY == 0 && (m + 1) / EVERY < NOP) {
        __builtin_amdgcn_sched_barrier(0);
        if (kt + NBUF - 1 < NK) stage_op(kt + NBUF - 1, (m + 1) / EVERY);
        __builtin_amdgcn_sched_barrier(0);
      }
    }
    __builtin_amdgcn_sched_barrier(0);
  }
  RAWBAR();
}

struct PGen {
  const u16* A; const u16* W; const u16* W2; int lda; int K; int ldb;
  __device__ const u16* a_row(int m) const { return A + (size_t)m * lda; }
  __device__ const u16* b_col(int n0, int sub) const { return W2 ? (sub ? W2 : W) + n0 : W + n0 + sub * 64; }
};

constexpr int PRE_MOD = 60, PRE_B = 32 * 5 * 5, PRE_WT = (2 * D_ * HD_ / 8) / 256;
__global__ void __launch_bounds__(256, 2) k_pre(const u16* __restrict__ c, const u16* __restrict__ wmod, const u16* __restrict__ mem0,
                                               const u16* __restrict__ Wq, const u16* __restrict__ Wk, const u16* __restrict__ Wv, const u16* __restrict__ Wo, char* ws) {
  extern __shared__ __attribute__((aligned(16))) char smem[];
  int b = blockIdx.x;
  const int lane = threadIdx.x & 63, w = threadIdx.x >> 6;
  if (b < PRE_MOD) {
    float* red = (float*)smem;
    float* sv = red + 256;
    for (int k = threadIdx.x; k < D_; k += 256) { float cv = bf2f(c[k]); sv[k] = rbf(cv / (1.f + __expf(-cv))); }
    __syncthreads();
    const int n = b * 64 + lane;
    float acc = 0.f;
#pragma unroll 8
    for (int k = w * 160; k < w * 160 + 160; ++k) acc += sv[k] * bf2f(wmod[(size_t)k * 3840 + n]);
    red[w * 64 + lane] = acc;
    __syncthreads();
    if (w == 0) ((float*)(ws + WS_MOD))[n] = rbf(red[lane] + red[64 + lane] + red[128 + lane] + red[192 + lane]);
    return;
  }
  b -= PRE_MOD;
  if (b < PRE_B) {
    constexpr int BM = 128, BN = 128;
    const int nb = b % 5; b /= 5; const int mb = b % 5; const int h = b / 5;
    PGen P; P.A = Wv + h * DH_; P.lda = HD_; P.W = Wo + (size_t)h * DH_ * D_; P.W2 = nullptr; P.K = DH_; P.ldb = D_;
    const int m0 = mb * BM, n0 = nb * BN;
    f32x4 acc[BM / 32][BN / 32];
    gemm_tile<BM, BN, false>(P, m0, n0, smem, acc);
    u16* U = (u16*)(ws + WS_U);
    const int wm = w >> 1, wn = w & 1, fr = lane & 15, g = lane >> 4;
#pragma unroll
    for (int i = 0; i < BM / 32; ++i)
#pragma unroll
      for (int j = 0; j < BN / 32; ++j) {
        int row = S_ + m0 + wm * (BM / 2) + i * 16 + 4 * g, cc = n0 + wn * (BN / 2) + j * 16 + fr;
        *(v2u*)(U + (((size_t)h * UCH_ + (row >> 3)) * D_ + cc) * 8 + (row & 7)) = pk4(acc[i][j]);
      }
    return;
  }
  b -= PRE_B;
  {
    const int gid = b * 256 + threadIdx.x;
    u16* memb = (u16*)(ws + WS_MEMB);
    u16* wt = (u16*)(ws + WS_WT);
    if (gid < 640) {
      int u = gid >> 3, i = gid & 7;
      *(v4u*)(memb + gid * 8) = *(const v4u*)(mem0 + i * D_ + u * 8);
    }
    int f = gid >> 6;
    const int ks = f % 20; f /= 20; const int p = f % 6; f /= 6; const int ww = f & 3; const int h = f >> 2;
    const u16* W = (p < 3) ? Wq : Wk;
    const int et = p % 3;
    const int e = (3 * ww + et) * 16 + (lane & 15);
    const int k0 = ks * 32 + 8 * (lane >> 4);
    u16 v[8];
#pragma unroll
    for (int j = 0; j < 8; ++j) v[j] = W[(size_t)(k0 + j) * HD_ + h * DH_ + e];
    v4u o = {v[0] | ((unsigned)v[1] << 16), v[2] | ((unsigned)v[3] << 16), v[4] | ((unsigned)v[5] << 16), v[6] | ((unsigned)v[7] << 16)};
    *(v4u*)(wt + (size_t)gid * 8) = o;
  }
}

struct MegaArgs {
  const u16* x; const u16* Wq; const u16* Wk; const u16* Wv; const u16* Wo; const u16* w1; const u16* w3; const u16* w2;
  char* ws; u16* out;
};
constexpr int MEGA_LDS = 160 * 1024;
#define USLEEP 8
#define STAMP(id)
#define TSTAMP(t, id)
#define MFMA_AA(acc, wa, mb) asm volatile("v_mfma_f32_16x16x32_bf16 %0, %1, %2, %0" : "+v"(acc) : "a"(wa), "v"(mb))
#define MFMA_VV(acc, wa, mb) asm volatile("v_mfma_f32_16x16x32_bf16 %0, %1, %2, %0" : "+v"(acc) : "v"(wa), "v"(mb))
#define MFMA_BA(acc, ma, wb) asm volatile("v_mfma_f32_16x16x32_bf16 %0, %1, %2, %0" : "+v"(acc) : "v"(ma), "a"(wb))
#define MFMA_ACCA(acc, ma, wb) asm("v_mfma_f32_16x16x32_bf16 %0, %1, %2, %0" : "+a"(acc) : "v"(ma), "v"(wb))
#define MFMA_ACCV(acc, ma, wb) asm("v_mfma_f32_16x16x32_bf16 %0, %1, %2, %0" : "+v"(acc) : "v"(ma), "v"(wb))
#define MFMA16(acc, ma, wb) asm volatile("v_mfma_f32_16x16x16_bf16 %0, %1, %2, %0" : "+v"(acc) : "v"(ma), "v"(wb))

#define PRE_NUM 3
__device__ __forceinline__ void presleep(unsigned long long t0, unsigned pre) {
  while ((unsigned)(__builtin_amdgcn_s_memtime() - t0) < pre) __builtin_amdgcn_s_sleep(2);
}
__device__ __forceinline__ unsigned nextpre(unsigned long long t0, unsigned& wmin) {
  unsigned wdur = (unsigned)(__builtin_amdgcn_s_memtime() - t0);
  wmin = min(wmin, wdur);
  return PRE_NUM * (wmin >> 3);
}
__device__ __forceinline__ float xmax8(float x) { return fmaxf(x, __builtin_bit_cast(float, __builtin_amdgcn_update_dpp(0, __builtin_bit_cast(int, x), 0x128, 0xf, 0xf, false))); }
__device__ __forceinline__ float xadd8(float x) { return x + __builtin_bit_cast(float, __builtin_amdgcn_update_dpp(0, __builtin_bit_cast(int, x), 0x128, 0xf, 0xf, false)); }
template <int N> __device__ __forceinline__ float dpp_ror_add(float x) { return x + __builtin_bit_cast(float, __builtin_amdgcn_update_dpp(0, __builtin_bit_cast(int, x), 0x120 + N, 0xf, 0xf, false)); }
__device__ __forceinline__ void swap16(float x, float& a, float& b) { auto r = __builtin_amdgcn_permlane16_swap(__float_as_uint(x), __float_as_uint(x), false, false); unsigned r0 = r[0], r1 = r[1]; a = __uint_as_float(r0); b = __uint_as_float(r1); }
__device__ __forceinline__ void swap32(float x, float& a, float& b) { auto r = __builtin_amdgcn_permlane32_swap(__float_as_uint(x), __float_as_uint(x), false, false); unsigned r0 = r[0], r1 = r[1]; a = __uint_as_float(r0); b = __uint_as_float(r1); }
__device__ __forceinline__ float xmax16(float x) { float a, b; swap16(x, a, b); return fmaxf(a, b); }
__device__ __forceinline__ float xadd16(float x) { float a, b; swap16(x, a, b); return a + b; }
__device__ __forceinline__ float xmax32(float x) { float a, b; swap32(x, a, b); return fmaxf(a, b); }
__device__ __forceinline__ float xadd32(float x) { float a, b; swap32(x, a, b); return a + b; }
__device__ __forceinline__ float wave_sum(float x) { x = dpp_ror_add<8>(x); x = dpp_ror_add<4>(x); x = dpp_ror_add<2>(x); x = dpp_ror_add<1>(x); return xadd32(xadd16(x)); }

__device__ __forceinline__ void score_role(const MegaArgs& a, char* smem, const int b) {
  const int tid = threadIdx.x, lane = tid & 63, w = __builtin_amdgcn_readfirstlane(tid >> 6);
  const int fr = lane & 15, g = lane >> 4;
  const __amdgpu_buffer_rsrc_t rm = __builtin_amdgcn_make_buffer_rsrc(a.ws + WS_MEMB, 0, 65 * 10240, 0x00020000);
  const __amdgpu_buffer_rsrc_t rp = __builtin_amdgcn_make_buffer_rsrc(a.ws + WS_PB, 0, 64 * 20480, 0x00020000);
  const __amdgpu_buffer_rsrc_t rkm = __builtin_amdgcn_make_buffer_rsrc(a.ws + WS_KM, 0, NSEG_ * 8 * HD_ * 2, 0x00020000);
  const u16* Kx = (const u16*)(a.ws + WS_K);
  unsigned* tmo = ctl(a.ws, C_TMO);
  const int moff = (g * 8 + (fr & 7)) * 16;
  const int h = b;
  char* lw = smem;
  float* part = (float*)(smem + 144 * 1024);
  bf16x8 wa[64], wv[20];
  const u16* wsrc = (const u16*)(a.ws + WS_WT) + ((size_t)(h * 4 + w) * 120 * 64 + lane) * 8;
#pragma unroll
  for (int f = 0; f < 64; ++f) wa[f] = *(const bf16x8*)(wsrc + (size_t)f * 512);
#pragma unroll
  for (int f = 0; f < 20; ++f) wv[f] = *(const bf16x8*)(wsrc + (size_t)(64 + f) * 512);
  for (int f = 0; f < 36; ++f) *(bf16x8*)(lw + (w * 36 + f) * 1024 + lane * 16) = *(const bf16x8*)(wsrc + (size_t)(84 + f) * 512);
  const char* lwp = lw + w * 36 * 1024 + lane * 16;
  const float scale = 0.07216878364870322f;
  unsigned pre_m = 0, wmin_m = 0xFFFFFFFFu;
  for (int s = 0; s < NSEG_; ++s) {
    if ((s & 3) == 0) {
      if (tid == 0) { spin_ge(ctl(a.ws, C_K + (s >> 2)), 32u, tmo); acquire_all(); }
      __syncthreads();
    }
    STAMP(0);
    bf16x4 kx[2][3];
#pragma unroll
    for (int kt = 0; kt < 2; ++kt)
#pragma unroll
      for (int et = 0; et < 3; ++et)
        kx[kt][et] = *(const bf16x4*)(Kx + (size_t)(s * 32 + kt * 16 + fr) * HD_ + h * DH_ + (3 * w + et) * 16 + 4 * g);
    bf16x8 l5[2];
    l5[0] = *(const bf16x8*)(lwp + 16 * 1024);
    l5[1] = *(const bf16x8*)(lwp + 17 * 1024);
    v4u mv[20];
    {
      unsigned spins = 0;
      const unsigned long long t0 = __builtin_amdgcn_s_memtime();
      presleep(t0, pre_m);
      for (;;) {
        unsigned mx = 0;
#pragma unroll
        for (int k = 0; k < 20; ++k) mv[k] = __builtin_amdgcn_raw_buffer_load_b128(rm, s * 10240 + k * 512 + moff, 0, 16);
#pragma unroll
        for (int k = 0; k < 20; ++k) mx = umax4(mx, mv[k]);
        if (!__any(mx == 0xFFFFFFFFu)) break;
        if (++spins > SPIN_CAP) { __hip_atomic_store(tmo, 1u, __ATOMIC_RELAXED, __HIP_MEMORY_SCOPE_AGENT); break; }
      }
      pre_m = nextpre(t0, wmin_m);
    }
    STAMP(1);
    f32x4 acc[6];
#pragma unroll
    for (int p = 0; p < 6; ++p) acc[p] = (f32x4){0.f, 0.f, 0.f, 0.f};
    bf16x8 l4[2];
    l4[0] = *(const bf16x8*)(lwp + 0 * 1024);
    l4[1] = *(const bf16x8*)(lwp + 1 * 1024);
    asm volatile("s_nop 1");
#pragma unroll
    for (int ks = 0; ks < 20; ++ks) {
      const bf16x8 mf = __builtin_bit_cast(bf16x8, mv[ks]);
      MFMA_AA(acc[0], wa[ks], mf);
      MFMA_AA(acc[1], wa[20 + ks], mf);
      MFMA_AA(acc[2], wa[40 + ks], mf);
      if (ks < 4) MFMA_AA(acc[3], wa[60 + ks], mf); else MFMA_VV(acc[3], wv[ks - 4], mf);
      if (ks < 4) MFMA_VV(acc[4], wv[16 + ks], mf);
      else { MFMA_VV(acc[4], l4[ks & 1], mf); if (ks + 2 < 20) l4[ks & 1] = *(const bf16x8*)(lwp + (ks + 2 - 4) * 1024); }
      MFMA_VV(acc[5], l5[ks & 1], mf);
      if (ks + 2 < 20) l5[ks & 1] = *(const bf16x8*)(lwp + (16 + ks + 2) * 1024);
    }
    asm volatile("s_nop 7\n\ts_nop 1" : "+v"(acc[0]), "+v"(acc[1]), "+v"(acc[2]), "+v"(acc[3]), "+v"(acc[4]), "+v"(acc[5]));
    STAMP(3);
    bf16x4 qf[3], kf[3];
#pragma unroll
    for (int et = 0; et < 3; ++et) { qf[et] = __builtin_bit_cast(bf16x4, pk4(acc[et])); kf[et] = __builtin_bit_cast(bf16x4, pk4(acc[3 + et])); }
    f32x4 st[3];
#pragma unroll
    for (int kt = 0; kt < 3; ++kt) st[kt] = (f32x4){0.f, 0.f, 0.f, 0.f};
    asm volatile("s_nop 1" : "+v"(qf[0]), "+v"(qf[1]), "+v"(qf[2]), "+v"(kf[0]), "+v"(kf[1]), "+v"(kf[2]), "+v"(st[0]), "+v"(st[1]), "+v"(st[2]));
#pragma unroll
    for (int et = 0; et < 3; ++et) {
      MFMA16(st[0], kf[et], qf[et]);
      MFMA16(st[1], kx[0][et], qf[et]);
      MFMA16(st[2], kx[1][et], qf[et]);
    }
    asm volatile("s_nop 7\n\ts_nop 1" : "+v"(st[0]), "+v"(st[1]), "+v"(st[2]));
    float* pt = part + (s & 1) * 1280;
    if (fr < 8) {
      if (g < 2) *(f32x4*)(pt + (w * 8 + fr) * 40 + 4 * g) = st[0];
      *(f32x4*)(pt + (w * 8 + fr) * 40 + 8 + 4 * g) = st[1];
      *(f32x4*)(pt + (w * 8 + fr) * 40 + 24 + 4 * g) = st[2];
    }
    STAMP(4);
    __syncthreads();
    STAMP(5);
    if (w == 0) {
      const int i = lane & 7, jc = lane >> 3;
      const int jr = jc < 5 ? jc : 0;
      float sc[8];
      f32x4 pp[8];
#pragma unroll
      for (int ww = 0; ww < 4; ++ww) { pp[2 * ww] = *(const f32x4*)(pt + (ww * 8 + i) * 40 + jr * 8); pp[2 * ww + 1] = *(const f32x4*)(pt + (ww * 8 + i) * 40 + jr * 8 + 4); }
      __builtin_amdgcn_sched_barrier(0);
#pragma unroll
      for (int e = 0; e < 4; ++e) { sc[e] = (pp[0][e] + pp[2][e]) + (pp[4][e] + pp[6][e]); sc[4 + e] = (pp[1][e] + pp[3][e]) + (pp[5][e] + pp[7][e]); }
      float mx = -3.0e38f;
#pragma unroll
      for (int e = 0; e < 8; ++e) { sc[e] = rbf(rbf(sc[e]) * scale); mx = fmaxf(mx, sc[e]); }
      mx = xmax8(mx); mx = xmax16(mx); mx = xmax32(mx);
      float sum = 0.f;
#pragma unroll
      for (int e = 0; e < 8; ++e) { sc[e] = __expf(sc[e] - mx); sum += sc[e]; }
      if (jc >= 5) sum = 0.f;
      sum = xadd8(sum); sum = xadd16(sum); sum = xadd32(sum);
      if (jc < 5) {
        const float inv = 1.f / sum;
        v4u o = {canon2(pack2(sc[0] * inv, sc[1] * inv)), canon2(pack2(sc[2] * inv, sc[3] * inv)), canon2(pack2(sc[4] * inv, sc[5] * inv)), canon2(pack2(sc[6] * inv, sc[7] * inv))};
        const int kc = (jc == 0) ? 128 + h : h * 4 + jc - 1;
        __builtin_amdgcn_raw_buffer_store_b128(o, rp, s * 20480 + kc * 128 + i * 16, 0, 16);
      }
    } else if (tid == 64 && s > 0) {
      __hip_atomic_fetch_add(ctl(a.ws, C_S + s - 1), 1u, __ATOMIC_RELAXED, __HIP_MEMORY_SCOPE_AGENT);
    }
    if (fr < 8) {
#pragma unroll
      for (int et = 0; et < 3; ++et)
        __builtin_amdgcn_raw_buffer_store_b64(__builtin_bit_cast(v2u, kf[et]), rkm, ((s * 8 + fr) * HD_ + h * DH_ + (3 * w + et) * 16 + 4 * g) * 2, 0, 16);
    }
    STAMP(6);
    __syncthreads();
  }
  publish(ctl(a.ws, C_S + NSEG_ - 1));
}

__device__ __forceinline__ void u_role(const MegaArgs& a, char* smem, const int b) {
  const int tid = threadIdx.x, lane = tid & 63, w = __builtin_amdgcn_readfirstlane(tid >> 6);
  const int fr = lane & 15, g = lane >> 4;
  const __amdgpu_buffer_rsrc_t rm = __builtin_amdgcn_make_buffer_rsrc(a.ws + WS_MEMB, 0, 65 * 10240, 0x00020000);
  const __amdgpu_buffer_rsrc_t rp = __builtin_amdgcn_make_buffer_rsrc(a.ws + WS_PB, 0, 64 * 20480, 0x00020000);
  const __amdgpu_buffer_rsrc_t rum = __builtin_amdgcn_make_buffer_rsrc(a.ws + WS_UM, 0, NSEG_ * H_ * D_ * 16, 0x00020000);
  const u16* U = (const u16*)(a.ws + WS_U);
  unsigned* tmo = ctl(a.ws, C_TMO);
  const int moff = (g * 8 + (fr & 7)) * 16;
  const int u = b - H_;
  char* lub = smem;
  float* red = (float*)(smem + 4096);
  bf16x8 ba[64], bv[16];
#pragma unroll
  for (int f = 0; f < 80; ++f) {
    const int nt = f / 20, ks = f % 20;
    const int hh = (4 * w + nt) * 2 + (fr >> 3), c = 8 * u + (fr & 7);
    bf16x8 v = *(const bf16x8*)(U + (((size_t)hh * UCH_ + 256 + ks * 4 + g) * D_ + c) * 8);
    if (f < 64) ba[f] = v; else bv[f - 64] = v;
  }
  unsigned pre_m = 0, pre_p = 0, wmin_m = 0xFFFFFFFFu, wmin_p = 0xFFFFFFFFu;
  for (int s = 0; s < NSEG_; ++s) {
    if ((s & 3) == 0) {
      if (tid == 0) { spin_ge(ctl(a.ws, C_K + (s >> 2)), 32u, tmo); acquire_all(); }
      __syncthreads();
    }
    STAMP(0);
    bf16x8 ub[10];
#pragma unroll
    for (int k = 0; k < 8; ++k) ub[k] = *(const bf16x8*)(U + (((size_t)(8 * w + k) * UCH_ + s * 4 + g) * D_ + 8 * u + (fr & 7)) * 8);
    v4u mv[20];
    {
      unsigned spins = 0;
      const unsigned long long t0 = __builtin_amdgcn_s_memtime();
      presleep(t0, pre_m);
      for (;;) {
        __builtin_amdgcn_s_sleep(USLEEP);
        unsigned mx = 0;
#pragma unroll
        for (int k = 0; k < 20; ++k) mv[k] = __builtin_amdgcn_raw_buffer_load_b128(rm, s * 10240 + k * 512 + moff, 0, 16);
#pragma unroll
        for (int k = 0; k < 20; ++k) mx = umax4(mx, mv[k]);
        if (!__any(mx == 0xFFFFFFFFu)) break;
        if (++spins > SPIN_CAP) { __hip_atomic_store(tmo, 1u, __ATOMIC_RELAXED, __HIP_MEMORY_SCOPE_AGENT); break; }
      }
      pre_m = nextpre(t0, wmin_m);
    }
    STAMP(1);
    {
      f32x4 acc[4];
#pragma unroll
      for (int nt = 0; nt < 4; ++nt) acc[nt] = (f32x4){0.f, 0.f, 0.f, 0.f};
      asm volatile("s_nop 1");
#pragma unroll
      for (int ks = 0; ks < 20; ++ks) {
        const bf16x8 mf = __builtin_bit_cast(bf16x8, mv[ks]);
        MFMA_BA(acc[0], mf, ba[ks]);
        MFMA_BA(acc[1], mf, ba[20 + ks]);
        MFMA_BA(acc[2], mf, ba[40 + ks]);
        if (ks < 4) MFMA_BA(acc[3], mf, ba[60 + ks]); else MFMA_VV(acc[3], mf, bv[ks - 4]);
      }
      asm volatile("s_nop 7\n\ts_nop 1" : "+v"(acc[0]), "+v"(acc[1]), "+v"(acc[2]), "+v"(acc[3]));
      if (g < 2) {
#pragma unroll
        for (int nt = 0; nt < 4; ++nt) {
          int hh = (4 * w + nt) * 2 + (fr >> 3), cc = fr & 7;
          v2u v = pk4(acc[nt]);
          *(v2u*)(lub + (hh * 8 + cc) * 16 + g * 8) = v;
          __builtin_amdgcn_raw_buffer_store_b64(v, rum, (((s * H_ + hh) * D_ + 8 * u + cc) * 8 + g * 4) * 2, 0, 16);
        }
      }
    }
    STAMP(2);
    __syncthreads();
    if (tid == 64 && s > 0) __hip_atomic_fetch_add(ctl(a.ws, C_S + s - 1), 1u, __ATOMIC_RELAXED, __HIP_MEMORY_SCOPE_AGENT);
#pragma unroll
    for (int k = 0; k < 2; ++k) ub[8 + k] = *(const bf16x8*)(lub + ((4 * (2 * w + k) + g) * 8 + (fr & 7)) * 16);
    STAMP(3);
    v4u pv[10];
    {
      unsigned spins = 0;
      const unsigned long long t0 = __builtin_amdgcn_s_memtime();
      presleep(t0, pre_p);
      for (;;) {
        unsigned mx = 0;
#pragma unroll
        for (int k = 0; k < 8; ++k) pv[k] = __builtin_amdgcn_raw_buffer_load_b128(rp, s * 20480 + (8 * w + k) * 512 + moff, 0, 16);
#pragma unroll
        for (int k = 0; k < 2; ++k) pv[8 + k] = __builtin_amdgcn_raw_buffer_load_b128(rp, s * 20480 + (32 + 2 * w + k) * 512 + moff, 0, 16);
#pragma unroll
        for (int k = 0; k < 10; ++k) mx = umax4(mx, pv[k]);
        if (!__any(mx == 0xFFFFFFFFu)) break;
        if (++spins > SPIN_CAP) { __hip_atomic_store(tmo, 1u, __ATOMIC_RELAXED, __HIP_MEMORY_SCOPE_AGENT); break; }
      }
      pre_p = nextpre(t0, wmin_p);
    }
    STAMP(4);
    f32x4 acc0 = {0.f, 0.f, 0.f, 0.f}, acc1 = {0.f, 0.f, 0.f, 0.f};
    asm volatile("s_nop 1" : "+v"(acc0), "+v"(acc1));
#pragma unroll
    for (int k = 0; k < 10; k += 2) {
      MFMA_VV(acc0, ub[k], __builtin_bit_cast(bf16x8, pv[k]));
      MFMA_VV(acc1, ub[k + 1], __builtin_bit_cast(bf16x8, pv[k + 1]));
    }
    asm volatile("s_nop 7\n\ts_nop 1" : "+v"(acc0), "+v"(acc1));
    acc0 += acc1;
    if (fr < 8 && g < 2) *(f32x4*)(red + (w * 8 + fr) * 8 + 4 * g) = acc0;
    __syncthreads();
    STAMP(5);
    if (tid < 16) {
      const int i = tid >> 1, hf = tid & 1;
      f32x4 o = *(const f32x4*)(red + (0 * 8 + i) * 8 + 4 * hf);
#pragma unroll
      for (int ww = 1; ww < 4; ++ww) o += *(const f32x4*)(red + (ww * 8 + i) * 8 + 4 * hf);
      v2u v = {canon2(pack2(o[0], o[1])), canon2(pack2(o[2], o[3]))};
      __builtin_amdgcn_raw_buffer_store_b64(v, rm, (s + 1) * 10240 + u * 128 + tid * 8, 0, 16);
    }
    STAMP(6);
  }
  publish(ctl(a.ws, C_S + NSEG_ - 1));
}

#define QKV_COMPUTE(BASE, KN) do { \
    char* base_ = (BASE); \
    bf16x8 af[4], bf[4]; \
    _Pragma("unroll") for (int i = 0; i < 4; ++i) { int row = wm * 64 + i * 16 + fr; af[i] = *(const bf16x8*)(base_ + row * 64 + ((g ^ (((row >> 3) & 1) * 3)) * 16)); } \
    _Pragma("unroll") for (int j = 0; j < 4; ++j) { const int nt = wn * 18 + j; bf[j] = tr_frag(base_ + 8192 + (nt >> 2) * 4096, 0, (nt & 3) * 2, lane); } \
    _Pragma("unroll") for (int j = 0; j < 18; ++j) { \
      __builtin_amdgcn_sched_barrier(0); \
      if ((KN) >= 0 && j * 11 / 17 != (j + 1) * 11 / 17 || ((KN) >= 0 && j == 0)) { } \
      _Pragma("unroll") for (int i = 0; i < 4; ++i) { if (i < 3 || j < 10) MFMA_ACCA(acc[i][j], bf[j & 3], af[i]); else MFMA_ACCV(acc[i][j], bf[j & 3], af[i]); } \
      __builtin_amdgcn_sched_barrier(0); \
      if (j + 4 < 18) { const int nt = wn * 18 + j + 4; bf[j & 3] = tr_frag(base_ + 8192 + (nt >> 2) * 4096, 0, (nt & 3) * 2, lane); } \
      if ((KN) >= 0) { const int o0 = (j * 11 + 17) / 18, o1 = ((j + 1) * 11 + 17) / 18; if (o1 > o0 && o0 < 11) stage_op((KN), o0); } \
    } } while (0)
__device__ __forceinline__ void task_qkvu(const MegaArgs& a, char* smem, int mt, int h) {
  constexpr int BM = 128;
  const int tid = otid(), lane = tid & 63, wid = tid >> 6, wm = wid >> 1, wn = wid & 1, fr = lane & 15, g = lane >> 4;
  char* vimg = smem + 96 * 1024;
  const u16* wosrc = a.Wo + (size_t)h * DH_ * D_ + (size_t)(tid >> 3) * D_ + (((tid & 7) ^ fswz(tid >> 3)) * 8);
  const unsigned ldsw = __builtin_amdgcn_readfirstlane((unsigned)(size_t)smem + (tid >> 6) * 1024);
  auto wo_op = [&](int pass, int it) {
    glds16(wosrc + (size_t)it * 32 * D_ + pass * 64, ldsw + (pass & 3) * 24576 + it * 4096);
  };
  auto wo_stage = [&](int pass) {
#pragma unroll
    for (int it = 0; it < 6; ++it) wo_op(pass, it);
  };
  {
    constexpr int STG = 8192 + 9 * 4096;
    const u16* asrc = (const u16*)(a.ws + WS_H1) + (size_t)(mt * BM + (tid >> 2)) * D_ + (((tid & 3) ^ (((tid >> 5) & 1) * 3)) * 8);
    const size_t boff = (size_t)(tid >> 3) * HD_ + h * DH_ + (((tid & 7) ^ fswz(tid >> 3)) * 8);
    auto stage_op = [&](int kt, int op) {
      const unsigned base = ldsw + (kt % 3) * STG;
      if (op < 2) glds16(asrc + (size_t)op * 64 * D_ + kt * 32, base + op * 4096);
      else {
        const int it = op - 2;
        const u16* W = (it < 3) ? a.Wq : (it < 6 ? a.Wk : a.Wv);
        glds16(W + boff + (size_t)(kt * 32) * HD_ + (it % 3) * 64, base + 8192 + it * 4096);
      }
    };
    f32x4 acc[4][18];
#pragma unroll
    for (int i = 0; i < 4; ++i)
#pragma unroll
      for (int j = 0; j < 18; ++j) acc[i][j] = (f32x4){0.f, 0.f, 0.f, 0.f};
#pragma unroll
    for (int op = 0; op < 11; ++op) stage_op(0, op);
#pragma unroll
    for (int op = 0; op < 11; ++op) stage_op(1, op);
    for (int kb = 0; kb < 18; kb += 3) {
#pragma unroll
      for (int d = 0; d < 3; ++d) {
        const int kt = kb + d;
        WAITV(11);
        RAWBAR();
        QKV_COMPUTE(smem + d * STG, kt + 2);
      }
    }
    WAITV(11); RAWBAR(); QKV_COMPUTE(smem + 0 * STG, -1);
    WAITV(0); RAWBAR(); QKV_COMPUTE(smem + 1 * STG, -1);
    RAWBAR();
#pragma unroll
    for (int j = 0; j < 18; ++j) asm volatile("s_nop 7\n\ts_nop 1" : "+a"(acc[0][j]), "+a"(acc[1][j]), "+a"(acc[2][j]));
#pragma unroll
    for (int j = 0; j < 10; ++j) asm volatile("s_nop 7\n\ts_nop 1" : "+a"(acc[3][j]));
#pragma unroll
    for (int j = 10; j < 18; ++j) asm volatile("s_nop 7\n\ts_nop 1" : "+v"(acc[3][j]));
    TSTAMP(mt * 32 + h, 3);
    wo_stage(0); wo_stage(1); wo_stage(2);
    const __amdgpu_buffer_rsrc_t rq = __builtin_amdgcn_make_buffer_rsrc(a.ws + WS_Q, 0, 2 * S_ * HD_ * 2, 0x00020000);
#pragma unroll
    for (int j = 0; j < 18; j += 2) {
      const int nt = wn * 18 + j;
      const int mat = nt / 12, nl = nt - mat * 12;
#pragma unroll
      for (int i = 0; i < 4; ++i) {
        if (mat < 2) {
          v4u v = pair16(pk4(acc[i][j]), pk4(acc[i][j + 1]));
          int tok = mt * BM + wm * 64 + i * 16 + fr, n = h * DH_ + (nl + (g & 1)) * 16 + (g >> 1) * 8;
          __builtin_amdgcn_raw_buffer_store_b128(v, rq, mat * (S_ * HD_ * 2) + (tok * HD_ + n) * 2, 0, 16);
        } else {
#pragma unroll
          for (int jj = 0; jj < 2; ++jj) {
            int row = wm * 64 + i * 16 + fr, n = (nl + jj) * 16 + 4 * g;
            *(v2u*)(vimg + (n >> 6) * 16384 + row * 128 + ((((n >> 3) & 7) ^ (row & 7)) * 16) + ((n >> 2) & 1) * 8) = pk4(acc[i][j + jj]);
          }
        }
      }
    }
  }
  const __amdgpu_buffer_rsrc_t ru = __builtin_amdgcn_make_buffer_rsrc(a.ws + WS_U, 0, H_ * UCH_ * D_ * 16, 0x00020000);
#pragma unroll
  for (int pass = 0; pass < 10; ++pass) {
    constexpr int N0[10] = {12, 16, 20, 24, 24, 24, 24, 24, 18, 12};
    if (pass < 3) { if (wn == 0) WAITV(N0[pass] + 36); else WAITV(N0[pass] + 12); } else WAITV(N0[pass]);
    RAWBAR();
    char* bb = smem + (pass & 3) * 24576;
    f32x4 acc[4][2];
#pragma unroll
    for (int i = 0; i < 4; ++i) { acc[i][0] = (f32x4){0.f, 0.f, 0.f, 0.f}; acc[i][1] = (f32x4){0.f, 0.f, 0.f, 0.f}; }
    bf16x8 af[2][4], bf[2][2];
    auto rdfr = [&](int ks, int sl) {
      const int kt = ks >> 1, kk = ks & 1;
#pragma unroll
      for (int j = 0; j < 2; ++j) bf[sl][j] = tr_frag(bb + kt * 8192, kk * 32, (wn * 32 + j * 16) >> 3, lane);
#pragma unroll
      for (int i = 0; i < 4; ++i) { int row = wm * 64 + i * 16 + fr; af[sl][i] = *(const bf16x8*)(vimg + kt * 16384 + row * 128 + (((kk * 4 + g) ^ (row & 7)) * 16)); }
    };
    rdfr(0, 0);
#pragma unroll
    for (int ks = 0; ks < 6; ++ks) {
      if (ks + 1 < 6) rdfr(ks + 1, (ks + 1) & 1);
      if (pass + 3 < 10) wo_op(pass + 3, ks);
      __builtin_amdgcn_sched_barrier(0);
#pragma unroll
      for (int i = 0; i < 4; ++i)
#pragma unroll
        for (int j = 0; j < 2; ++j) acc[i][j] = __builtin_amdgcn_mfma_f32_16x16x32_bf16(af[ks & 1][i], bf[ks & 1][j], acc[i][j], 0, 0, 0);
      __builtin_amdgcn_sched_barrier(0);
    }
#pragma unroll
    for (int i = 0; i < 4; i += 2)
#pragma unroll
      for (int j = 0; j < 2; ++j) {
        v4u v = pair16(pk4(acc[i][j]), pk4(acc[i + 1][j]));
        int row = mt * BM + wm * 64 + (i + (g & 1)) * 16 + (g >> 1) * 8, c = pass * 64 + wn * 32 + j * 16 + fr;
        __builtin_amdgcn_raw_buffer_store_b128(v, ru, ((h * UCH_ + (row >> 3)) * D_ + c) * 16, 0, 16);
      }
  }
}

__device__ __forceinline__ void task_x(const MegaArgs& a, char* smem, int s, int hq) {
  const int tid = otid(), lane = tid & 63, w = tid >> 6, fr = lane & 15, g = lane >> 4;
  const u16* Q = (const u16*)(a.ws + WS_Q);
  const u16* Kx = (const u16*)(a.ws + WS_K);
  const u16* km = (const u16*)(a.ws + WS_KM);
  const u16* U = (const u16*)(a.ws + WS_U);
  const u16* Um = (const u16*)(a.ws + WS_UM);
  constexpr int PSTR = 656;
  const float scale = 0.07216878364870322f;
  bf16x8 kfa[2][6][3], qfa[2][6][2];
#pragma unroll
  for (int hh = 0; hh < 2; ++hh)
#pragma unroll
    for (int ks = 0; ks < 6; ++ks) {
      const int eo = (8 * hq + 2 * w + hh) * DH_ + ks * 32 + 8 * g;
      kfa[hh][ks][0] = *(const bf16x8*)(km + ((size_t)s * 8 + (fr & 7)) * HD_ + eo);
      kfa[hh][ks][1] = *(const bf16x8*)(Kx + (size_t)(s * 32 + fr) * HD_ + eo);
      kfa[hh][ks][2] = *(const bf16x8*)(Kx + (size_t)(s * 32 + 16 + fr) * HD_ + eo);
      qfa[hh][ks][0] = *(const bf16x8*)(Q + (size_t)(s * 32 + fr) * HD_ + eo);
      qfa[hh][ks][1] = *(const bf16x8*)(Q + (size_t)(s * 32 + 16 + fr) * HD_ + eo);
    }
  __builtin_amdgcn_sched_barrier(0);
#pragma unroll
  for (int hh = 0; hh < 2; ++hh) {
    const int hl = 2 * w + hh;
    f32x4 acc[3][2];
#pragma unroll
    for (int kt = 0; kt < 3; ++kt) { acc[kt][0] = (f32x4){0.f, 0.f, 0.f, 0.f}; acc[kt][1] = (f32x4){0.f, 0.f, 0.f, 0.f}; }
#pragma unroll
    for (int ks = 0; ks < 6; ++ks)
#pragma unroll
      for (int kt = 0; kt < 3; ++kt)
#pragma unroll
        for (int tt = 0; tt < 2; ++tt) acc[kt][tt] = __builtin_amdgcn_mfma_f32_16x16x32_bf16(kfa[hh][ks][kt], qfa[hh][ks][tt], acc[kt][tt], 0, 0, 0);
#pragma unroll
    for (int tt = 0; tt < 2; ++tt) {
      const int t = tt * 16 + fr;
      float sc[12]; bool ok[12];
      float mx = -3.0e38f;
#pragma unroll
      for (int kt = 0; kt < 3; ++kt)
#pragma unroll
        for (int r = 0; r < 4; ++r) {
          int jl = 4 * g + r;
          bool v = (kt == 0) ? (jl < 8) : ((kt - 1) * 16 + jl <= t);
          float xx = rbf(rbf(acc[kt][tt][r]) * scale);
          sc[kt * 4 + r] = xx; ok[kt * 4 + r] = v;
          if (v) mx = fmaxf(mx, xx);
        }
      mx = xmax16(mx); mx = xmax32(mx);
      float sum = 0.f;
#pragma unroll
      for (int e = 0; e < 12; ++e) { sc[e] = ok[e] ? __expf(sc[e] - mx) : 0.f; sum += sc[e]; }
      sum = xadd16(sum); sum = xadd32(sum);
      const float inv = 1.f / sum;
      char* dst = smem + t * PSTR + hl * 80;
      if (g < 2) *(v2u*)(dst + 8 * g) = (v2u){pack2(sc[0] * inv, sc[1] * inv), pack2(sc[2] * inv, sc[3] * inv)};
      *(v2u*)(dst + 16 + 8 * g) = (v2u){pack2(sc[4] * inv, sc[5] * inv), pack2(sc[6] * inv, sc[7] * inv)};
      *(v2u*)(dst + 48 + 8 * g) = (v2u){pack2(sc[8] * inv, sc[9] * inv), pack2(sc[10] * inv, sc[11] * inv)};
    }
  }
  bf16x8 uf[4][10];
  auto uload = [&](int ks) {
    const int kc = ks * 4 + g, hl = kc / 5, jc = kc - hl * 5, h = 8 * hq + hl;
    const u16* ub = ((jc == 0) ? Um + (((size_t)s * H_ + h) * D_) * 8 : U + (((size_t)h * UCH_ + s * 4 + jc - 1) * D_) * 8) + (size_t)(160 * w + fr) * 8;
#pragma unroll
    for (int ct = 0; ct < 10; ++ct) uf[ks & 3][ct] = *(const bf16x8*)(ub + ct * 128);
  };
  uload(0); uload(1); uload(2);
  __syncthreads();
  f32x4 acc[10][2];
#pragma unroll
  for (int ct = 0; ct < 10; ++ct) { acc[ct][0] = (f32x4){0.f, 0.f, 0.f, 0.f}; acc[ct][1] = (f32x4){0.f, 0.f, 0.f, 0.f}; }
#pragma unroll
  for (int ks = 0; ks < 10; ++ks) {
    if (ks + 3 < 10) uload(ks + 3);
    bf16x8 pf[2];
    pf[0] = *(const bf16x8*)(smem + fr * PSTR + (ks * 4 + g) * 16);
    pf[1] = *(const bf16x8*)(smem + (16 + fr) * PSTR + (ks * 4 + g) * 16);
    __builtin_amdgcn_sched_barrier(0);
#pragma unroll
    for (int ct = 0; ct < 10; ++ct)
#pragma unroll
      for (int tt = 0; tt < 2; ++tt) acc[ct][tt] = __builtin_amdgcn_mfma_f32_16x16x32_bf16(uf[ks & 3][ct], pf[tt], acc[ct][tt], 0, 0, 0);
    __builtin_amdgcn_sched_barrier(0);
  }
  const __amdgpu_buffer_rsrc_t rx = __builtin_amdgcn_make_buffer_rsrc(a.ws + WS_XP, 0, NSEG_ * 4 * 32 * D_ * 4, 0x00020000);
#pragma unroll
  for (int ct = 0; ct < 10; ++ct)
#pragma unroll
    for (int tt = 0; tt < 2; ++tt) {
      const int t = tt * 16 + fr, c = 160 * w + ct * 16 + 4 * g;
      __builtin_amdgcn_raw_buffer_store_b128(__builtin_bit_cast(v4u, acc[ct][tt]), rx, (((s * 4 + hq) * 32 + t) * D_ + c) * 4, 0, 16);
    }
}

__device__ __forceinline__ void task_xc(const MegaArgs& a, char* smem, int s) {
  const int tid = otid(), lane = tid & 63, w = tid >> 6;
  const float* mod = (const float*)(a.ws + WS_MOD);
  const float* xp = (const float*)(a.ws + WS_XP) + (size_t)s * 4 * 32 * D_;
  const __amdgpu_buffer_rsrc_t rx1 = __builtin_amdgcn_make_buffer_rsrc(a.ws + WS_X1, 0, S_ * D_ * 2, 0x00020000);
  const __amdgpu_buffer_rsrc_t rh2 = __builtin_amdgcn_make_buffer_rsrc(a.ws + WS_H2, 0, S_ * D_ * 2, 0x00020000);
  float* xl = (float*)smem + w * 8 * D_;
#pragma unroll
  for (int hb = 0; hb < 2; ++hb) {
    f32x4 pp[5][4][2]; v4u xx[5]; f32x4 gg[5][2];
#pragma unroll
    for (int k = 0; k < 5; ++k) {
      const int q = (hb * 5 + k) * 64 + lane, row = q / 80, c0 = (q - row * 80) * 8, t = 8 * w + row;
#pragma unroll
      for (int pq = 0; pq < 4; ++pq) { pp[k][pq][0] = *(const f32x4*)(xp + ((size_t)pq * 32 + t) * D_ + c0); pp[k][pq][1] = *(const f32x4*)(xp + ((size_t)pq * 32 + t) * D_ + c0 + 4); }
      xx[k] = *(const v4u*)(a.x + (size_t)(s * 32 + t) * D_ + c0);
      gg[k][0] = *(const f32x4*)(mod + 2 * D_ + c0); gg[k][1] = *(const f32x4*)(mod + 2 * D_ + c0 + 4);
    }
    __builtin_amdgcn_sched_barrier(0);
#pragma unroll
    for (int k = 0; k < 5; ++k) {
      const int q = (hb * 5 + k) * 64 + lane;
      f32x4 s0 = (pp[k][0][0] + pp[k][1][0]) + (pp[k][2][0] + pp[k][3][0]);
      f32x4 s1 = (pp[k][0][1] + pp[k][1][1]) + (pp[k][2][1] + pp[k][3][1]);
      f32x4 o0, o1;
#pragma unroll
      for (int e = 0; e < 4; ++e) {
        float x0 = (e & 1) ? bfhi(xx[k][e >> 1]) : bflo(xx[k][e >> 1]);
        float x1v = (e & 1) ? bfhi(xx[k][2 + (e >> 1)]) : bflo(xx[k][2 + (e >> 1)]);
        o0[e] = rbf(x0 + rbf(gg[k][0][e] * rbf(s0[e])));
        o1[e] = rbf(x1v + rbf(gg[k][1][e] * rbf(s1[e])));
      }
      *(f32x4*)(xl + q * 8) = o0; *(f32x4*)(xl + q * 8 + 4) = o1;
    }
  }
  const bool two = lane < 16;
  const int c1 = two ? 512 + lane * 8 : lane * 8;
  f32x4 msb[2][2], mcb[2][2];
#pragma unroll
  for (int part = 0; part < 2; ++part) {
    const int c0 = part ? c1 : lane * 8;
#pragma unroll
    for (int hf = 0; hf < 2; ++hf) { msb[part][hf] = *(const f32x4*)(mod + 3 * D_ + c0 + 4 * hf); mcb[part][hf] = *(const f32x4*)(mod + 4 * D_ + c0 + 4 * hf); }
  }
#pragma unroll 4
  for (int rr = 0; rr < 8; ++rr) {
    const int t = 8 * w + rr;
    float v[16];
    {
      f32x4 p0 = *(const f32x4*)(xl + rr * D_ + lane * 8), p1 = *(const f32x4*)(xl + rr * D_ + lane * 8 + 4);
      f32x4 p2 = *(const f32x4*)(xl + rr * D_ + c1), p3 = *(const f32x4*)(xl + rr * D_ + c1 + 4);
#pragma unroll
      for (int e = 0; e < 4; ++e) { v[e] = p0[e]; v[4 + e] = p1[e]; v[8 + e] = two ? p2[e] : 0.f; v[12 + e] = two ? p3[e] : 0.f; }
    }
    float sm = 0.f;
#pragma unroll
    for (int e = 0; e < 16; ++e) sm += v[e];
    sm = wave_sum(sm);
    const float mean = sm * (1.f / D_);
    float q = 0.f;
#pragma unroll
    for (int e = 0; e < 8; ++e) { float d = v[e] - mean; q += d * d; }
    if (two) {
#pragma unroll
      for (int e = 8; e < 16; ++e) { float d = v[e] - mean; q += d * d; }
    }
    q = wave_sum(q);
    const float mb = rbf(mean), vb = rbf(q * (1.f / D_));
    const float den = rbf(sqrtf(rbf(vb + 1e-5f)));
#pragma unroll
    for (int part = 0; part < 2; ++part) {
      if (part == 0 || two) {
        const int c0 = part * 512 + lane * 8;
        float o[8];
#pragma unroll
        for (int e = 0; e < 8; ++e) {
          float tv = rbf(rbf(v[part * 8 + e] - mb) / den);
          tv = rbf(tv * rbf(1.f + mcb[part][e >> 2][e & 3]));
          o[e] = tv + msb[part][e >> 2][e & 3];
        }
        const int off = ((s * 32 + t) * D_ + c0) * 2;
        v4u hv = {pack2(o[0], o[1]), pack2(o[2], o[3]), pack2(o[4], o[5]), pack2(o[6], o[7])};
        v4u xv = {pack2(v[part * 8 + 0], v[part * 8 + 1]), pack2(v[part * 8 + 2], v[part * 8 + 3]), pack2(v[part * 8 + 4], v[part * 8 + 5]), pack2(v[part * 8 + 6], v[part * 8 + 7])};
        __builtin_amdgcn_raw_buffer_store_b128(hv, rh2, off, 0, 16);
        __builtin_amdgcn_raw_buffer_store_b128(xv, rx1, off, 0, 16);
      }
    }
  }
}

__device__ __forceinline__ void task_f1(const MegaArgs& a, char* smem, int s, int nb) {
  constexpr int BM = 32, BN = 128;
  PGen P; P.A = (const u16*)(a.ws + WS_H2); P.lda = D_; P.W = a.w1; P.W2 = a.w3; P.K = D_; P.ldb = F_;
  const int m0 = s * BM, f0 = nb * 64;
  f32x4 acc[BM / 32][BN / 32];
  gemm_tile_dma<BM, BN, 10, 6, true>(P, m0, f0, smem, acc);
  const int tid = otid(); const int lane = tid & 63, wid = tid >> 6, wm = wid >> 1, wn = wid & 1, fr = lane & 15, g = lane >> 4;
  f32x4* ex = (f32x4*)smem;
  if (wn == 1) {
#pragma unroll
    for (int j = 0; j < BN / 32; ++j) ex[(wm * (BN / 32) + j) * 64 + lane] = acc[0][j];
  }
  __syncthreads();
  if (wn == 0) {
    const __amdgpu_buffer_rsrc_t rg = __builtin_amdgcn_make_buffer_rsrc(a.ws + WS_G, 0, S_ * F_ * 2, 0x00020000);
#pragma unroll
    for (int j = 0; j < BN / 32; j += 2) {
      v2u pk[2];
#pragma unroll
      for (int jj = 0; jj < 2; ++jj) {
        f32x4 up = ex[(wm * (BN / 32) + j + jj) * 64 + lane];
        f32x4 o;
#pragma unroll
        for (int r = 0; r < 4; ++r) {
          float gt = rbf(acc[0][j + jj][r]);
          float sg = rbf(gt / (1.f + __expf(-gt)));
          o[r] = sg * rbf(up[r]);
        }
        pk[jj] = pk4(o);
      }
      v4u v = pair16(pk[0], pk[1]);
      int tok = m0 + wm * (BM / 2) + fr, f = f0 + (j + (g & 1)) * 16 + (g >> 1) * 8;
      __builtin_amdgcn_raw_buffer_store_b128(v, rg, (tok * F_ + f) * 2, 0, 16);
    }
  }
  __syncthreads();
}

__device__ __forceinline__ void task_f2(const MegaArgs& a, char* smem, int s, int nb) {
  constexpr int BM = 32, BN = 64;
  PGen P; P.A = (const u16*)(a.ws + WS_G); P.lda = F_; P.W = a.w2; P.W2 = nullptr; P.K = F_; P.ldb = D_;
  const int m0 = s * BM, n0 = nb * BN;
  f32x4 acc[BM / 32][BN / 32];
  gemm_tile_dma<BM, BN, 24, 8, true>(P, m0, n0, smem, acc);
  const int tid = otid(); const int lane = tid & 63, wid = tid >> 6, wm = wid >> 1, wn = wid & 1, fr = lane & 15, g = lane >> 4;
  const u16* x1 = (const u16*)(a.ws + WS_X1);
  const float* gb = (const float*)(a.ws + WS_MOD) + 5 * D_;
#pragma unroll
  for (int j = 0; j < BN / 32; ++j) {
    int tok = m0 + wm * (BM / 2) + fr, c = n0 + wn * (BN / 2) + j * 16 + 4 * g;
    v2u xv = *(const v2u*)(x1 + (size_t)tok * D_ + c);
    float xo[4] = {bflo(xv[0]), bfhi(xv[0]), bflo(xv[1]), bfhi(xv[1])};
    f32x4 o;
#pragma unroll
    for (int r = 0; r < 4; ++r) o[r] = xo[r] + rbf(gb[c + r] * rbf(acc[0][j][r]));
    *(v2u*)(a.out + (size_t)tok * D_ + c) = pk4(o);
  }
}

constexpr int NP_S = 4 + 24 + 10;
constexpr int D1_ = 8, D2_ = 10;
constexpr int NT2_ = (NSEG_ + D2_) * NP_S;
__device__ __forceinline__ void worker_role(const MegaArgs& a, char* smem, const int x, const int j) {
  unsigned* tmo = ctl(a.ws, C_TMO);
  {
    const int nh = (x == 3) ? 4 : 7, h0 = (x == 3) ? 0 : 4 + (x - 4) * 7, nw = (x == 3) ? 16 : 32;
    for (int e = j; e < NMT_ * nh; e += nw) {
      const int mt = e / nh, h = h0 + e % nh;
      TSTAMP(mt * 32 + h, 0); TSTAMP(mt * 32 + h, 1);
      task_qkvu(a, smem, mt, h);
      publish(ctl(a.ws, C_K + mt));
      TSTAMP(mt * 32 + h, 2);
    }
  }
  volatile unsigned* lw = (volatile unsigned*)smem;
  for (;;) {
    __syncthreads();
    if (threadIdx.x == 0) lw[0] = __hip_atomic_fetch_add(ctl(a.ws, C_QUEUE), 1u, __ATOMIC_RELAXED, __HIP_MEMORY_SCOPE_AGENT);
    __syncthreads();
    const int t = __builtin_amdgcn_readfirstlane((int)lw[0]);
    __syncthreads();
    if (t >= NT2_) break;
    const int r = t % NP_S;
    const int s = t / NP_S - (r < 4 ? 0 : (r < 28 ? D1_ : D2_));
    if (s < 0 || s >= NSEG_) continue;
    TSTAMP(512 + s * NP_S + r, 0);
    if (r < 4) {
      const int hq = r;
      if (threadIdx.x == 0) { spin_ge(ctl(a.ws, C_K + (s >> 2)), 32u, tmo); spin_ge(ctl(a.ws, C_S + s), (unsigned)NCHAIN_, tmo); acquire_all(); }
      __syncthreads();
      TSTAMP(512 + s * NP_S + r, 1);
      task_x(a, smem, s, hq);
      asm volatile("s_waitcnt vmcnt(0)" ::: "memory");
      __syncthreads();
      if (threadIdx.x == 0) {
        unsigned old = __hip_atomic_fetch_add(ctl(a.ws, C_T + s), 1u, __ATOMIC_RELAXED, __HIP_MEMORY_SCOPE_AGENT);
        if (old == 3u) acquire_all();
        lw[0] = old;
      }
      __syncthreads();
      const unsigned old = (unsigned)__builtin_amdgcn_readfirstlane((int)lw[0]);
      __syncthreads();
      if (old == 3u) {
        TSTAMP(512 + s * NP_S + r, 3);
        task_xc(a, smem, s);
        publish(ctl(a.ws, C_XS + s));
      }
    } else if (r < 28) {
      if (threadIdx.x == 0) { spin_ge(ctl(a.ws, C_XS + s), 1u, tmo); acquire_all(); }
      __syncthreads();
      TSTAMP(512 + s * NP_S + r, 1);
      task_f1(a, smem, s, r - 4);
      publish(ctl(a.ws, C_FS + s));
    } else {
      if (threadIdx.x == 0) { spin_ge(ctl(a.ws, C_FS + s), 24u, tmo); acquire_all(); }
      __syncthreads();
      TSTAMP(512 + s * NP_S + r, 1);
      task_f2(a, smem, s, r - 28);
      __syncthreads();
    }
    TSTAMP(512 + s * NP_S + r, 2);
  }
}

__global__ void __launch_bounds__(256, 1) k_mega(MegaArgs a) {
  extern __shared__ __attribute__((aligned(16))) char smem[];
  const int x = blockIdx.x & 7, j = blockIdx.x >> 3;
  if (x < 3 || (x == 3 && j < 16)) {
    const int cidx = (x < 3) ? x * 32 + j : 96 + j;
    if (cidx < H_) score_role(a, smem, cidx);
    else u_role(a, smem, cidx);
  } else {
    worker_role(a, smem, x, x == 3 ? j - 16 : j);
  }
}

extern "C" void kernel_launch(void* const* d_in, const int* in_sizes, int n_in,
                              void* d_out, int out_size, void* d_ws, size_t ws_size,
                              hipStream_t stream) {
  const u16* x = (const u16*)d_in[0];
  const u16* mem0 = (const u16*)d_in[1];
  const u16* Wq = (const u16*)d_in[2];
  const u16* Wk = (const u16*)d_in[3];
  const u16* Wv = (const u16*)d_in[4];
  const u16* Wo = (const u16*)d_in[5];
  const u16* w1 = (const u16*)d_in[8];
  const u16* w3 = (const u16*)d_in[9];
  const u16* w2 = (const u16*)d_in[10];
  const u16* c = (const u16*)d_in[11];
  const u16* wmod = (const u16*)d_in[12];
  char* ws = (char*)d_ws;
  float* mod = (float*)(ws + WS_MOD);

  (void)hipFuncSetAttribute((const void*)k_pre, hipFuncAttributeMaxDynamicSharedMemorySize, 2 * (128 + 128) * 128);
  (void)hipFuncSetAttribute((const void*)k_mega, hipFuncAttributeMaxDynamicSharedMemorySize, MEGA_LDS);

  (void)hipMemsetAsync(ws + WS_CTL, 0, CTL_BYTES, stream);
  (void)hipMemsetAsync(ws + WS_MEMB, 0xFF, WS_SENT_END - WS_MEMB, stream);
  k_pre<<<PRE_MOD + PRE_B + PRE_WT, 256, 2 * (128 + 128) * 128, stream>>>(c, wmod, mem0, Wq, Wk, Wv, Wo, ws);
  k_ln<<<S_ / 4, 256, 0, stream>>>(x, mod + 0 * D_, mod + 1 * D_, (u16*)(ws + WS_H1));
  MegaArgs ma; ma.x = x; ma.Wq = Wq; ma.Wk = Wk; ma.Wv = Wv; ma.Wo = Wo; ma.w1 = w1; ma.w3 = w3; ma.w2 = w2; ma.ws = ws; ma.out = (u16*)d_out;
  k_mega<<<NBLK_, 256, MEGA_LDS, stream>>>(ma);
}
```

```cpp
#include <hip/hip_runtime.h>
#include <hip/hip_bf16.h>
#include <stdint.h>

constexpr int S_ = 2048, D_ = 640, H_ = 32, DH_ = 192, NSEG_ = 64, F_ = 1536;
constexpr int HD_ = H_ * DH_;
constexpr int NU_ = 80;
constexpr int UROWS_ = S_ + D_;
constexpr int UCH_ = UROWS_ / 8;
constexpr int NCHAIN_ = H_ + NU_;
constexpr int NBLK_ = 256;
constexpr int NWRK_ = NBLK_ - NCHAIN_;
constexpr int NMT_ = 16;

typedef short bf16x8 __attribute__((ext_vector_type(8)));
typedef short bf16x4 __attribute__((ext_vector_type(4)));
typedef float f32x4 __attribute__((ext_vector_type(4)));
typedef unsigned v4u __attribute__((ext_vector_type(4)));
typedef unsigned v2u __attribute__((ext_vector_type(2)));
typedef unsigned short u16;
typedef short v4i16_t __attribute__((ext_vector_type(4)));

__device__ __forceinline__ float bf2f(u16 x) { return __uint_as_float(((unsigned)x) << 16); }
__device__ __forceinline__ float bflo(unsigned x) { return __uint_as_float(x << 16); }
__device__ __forceinline__ float bfhi(unsigned x) { return __uint_as_float(x & 0xFFFF0000u); }
__device__ __forceinline__ u16 f2bf(float f) { return __builtin_bit_cast(u16, (__bf16)f); }
__device__ __forceinline__ float rbf(float f) { return bf2f(f2bf(f)); }
typedef __bf16 bf2_t __attribute__((ext_vector_type(2)));
typedef float f2_t __attribute__((ext_vector_type(2)));
__device__ __forceinline__ unsigned pack2(float a, float b) { f2_t f = {a, b}; bf2_t r = __builtin_convertvector(f, bf2_t); return __builtin_bit_cast(unsigned, r); }
__device__ __forceinline__ unsigned canon2(unsigned x) { return x == 0xFFFFFFFFu ? 0xFFFFFFFEu : x; }
__device__ __forceinline__ unsigned umax4(unsigned m, v4u v) {
  m = max(max(m, v[0]), v[1]);
  return max(max(m, v[2]), v[3]);
}

constexpr size_t al256(size_t x) { return (x + 255) & ~(size_t)255; }
constexpr size_t WS_CTL  = 0;
constexpr size_t CTL_BYTES = 32768;
constexpr size_t WS_MEMB = CTL_BYTES;
constexpr size_t WS_PB   = WS_MEMB + (size_t)65 * 10240;
constexpr size_t WS_SENT_END = WS_PB + (size_t)64 * 20480;
constexpr size_t WS_MOD  = al256(WS_SENT_END);
constexpr size_t WS_H1   = al256(WS_MOD + 3840 * 4);
constexpr size_t WS_Q    = al256(WS_H1 + (size_t)S_ * D_ * 2);
constexpr size_t WS_K    = WS_Q + (size_t)S_ * HD_ * 2;
constexpr size_t WS_U    = WS_K + (size_t)S_ * HD_ * 2;
constexpr size_t WS_WT   = WS_U + (size_t)H_ * UCH_ * D_ * 8 * 2;
constexpr size_t WS_KM   = WS_WT + (size_t)2 * D_ * HD_ * 2;
constexpr size_t WS_UM   = WS_KM + (size_t)NSEG_ * 8 * HD_ * 2;
constexpr size_t WS_X1   = WS_UM + (size_t)NSEG_ * H_ * D_ * 8 * 2;
constexpr size_t WS_H2   = WS_X1 + (size_t)S_ * D_ * 2;
constexpr size_t WS_G    = WS_H2 + (size_t)S_ * D_ * 2;
constexpr size_t WS_XP   = WS_G + (size_t)S_ * F_ * 2;
constexpr size_t WS_END  = WS_XP + (size_t)NSEG_ * 8 * 32 * D_ * 4;
constexpr int C_TMO = 0, C_K = 1, C_X = 49, C_F = 65, C_S = 81, C_T = 145, C_QUEUE = 210, C_XS = 256, C_FS = 320, C_Q0 = 211, C_B = 213;
__device__ __forceinline__ unsigned* ctl(char* ws, int idx) { return (unsigned*)(ws + WS_CTL) + idx * 16; }

constexpr unsigned SPIN_CAP = 1u << 22;

__device__ __forceinline__ void spin_ge(unsigned* c, unsigned target, unsigned* tmo) {
  unsigned spins = 0;
  while (__hip_atomic_load(c, __ATOMIC_RELAXED, __HIP_MEMORY_SCOPE_AGENT) < target) {
    __builtin_amdgcn_s_sleep(2);
    if (++spins > SPIN_CAP) { __hip_atomic_store(tmo, 1u, __ATOMIC_RELAXED, __HIP_MEMORY_SCOPE_AGENT); break; }
  }
}
__device__ __forceinline__ void acquire_all() {
  __builtin_amdgcn_fence(__ATOMIC_ACQUIRE, "agent");
  asm volatile("s_waitcnt vmcnt(0)" ::: "memory");
}
__device__ __forceinline__ void publish(unsigned* c) {
  asm volatile("s_waitcnt vmcnt(0)" ::: "memory");
  __syncthreads();
  if (threadIdx.x == 64) __hip_atomic_fetch_add(c, 1u, __ATOMIC_RELAXED, __HIP_MEMORY_SCOPE_AGENT);
  __syncthreads();
}

__device__ __forceinline__ v4u pair16(v2u A, v2u B) {
  auto r0 = __builtin_amdgcn_permlane16_swap(A[0], B[0], false, false);
  auto r1 = __builtin_amdgcn_permlane16_swap(A[1], B[1], false, false);
  unsigned a0 = r0[0], b0 = r0[1], a1 = r1[0], b1 = r1[1];
  return (v4u){a0, a1, b0, b1};
}
__device__ __forceinline__ int otid() { int t = threadIdx.x; asm volatile("" : "+v"(t)); return t; }
__device__ __forceinline__ v2u pk4(f32x4 v) { return (v2u){pack2(v[0], v[1]), pack2(v[2], v[3])}; }

__global__ void __launch_bounds__(256) k_ln(const u16* __restrict__ x, const float* __restrict__ shift, const float* __restrict__ scale, u16* __restrict__ y) {
  const int lane = threadIdx.x & 63, w = threadIdx.x >> 6;
  const int row = blockIdx.x * 4 + w;
  const unsigned* xr = (const unsigned*)(x + (size_t)row * D_);
  float v[10];
  float s = 0.f;
#pragma unroll
  for (int i = 0; i < 5; ++i) { unsigned u = xr[lane + 64 * i]; v[2 * i] = bflo(u); v[2 * i + 1] = bfhi(u); s += v[2 * i] + v[2 * i + 1]; }
#pragma unroll
  for (int m = 32; m >= 1; m >>= 1) s += __shfl_xor(s, m);
  const float mean = s * (1.f / D_);
  float q = 0.f;
#pragma unroll
  for (int i = 0; i < 10; ++i) { float d = v[i] - mean; q += d * d; }
#pragma unroll
  for (int m = 32; m >= 1; m >>= 1) q += __shfl_xor(q, m);
  const float mb = rbf(mean), vb = rbf(q * (1.f / D_));
  const float den = rbf(sqrtf(rbf(vb + 1e-5f)));
  unsigned* yr = (unsigned*)(y + (size_t)row * D_);
#pragma unroll
  for (int i = 0; i < 5; ++i) {
    int c = 2 * (lane + 64 * i);
    float o[2];
#pragma unroll
    for (int e = 0; e < 2; ++e) {
      float t = rbf(rbf(v[2 * i + e] - mb) / den);
      t = rbf(t * rbf(1.f + scale[c + e]));
      o[e] = t + shift[c + e];
    }
    yr[lane + 64 * i] = pack2(o[0], o[1]);
  }
}

__device__ __forceinline__ int fswz(int r) { return (((r >> 1) & 1) << 1) | (((r >> 3) & 1) << 2); }

__device__ __forceinline__ bf16x8 tr_frag(const char* sub_base, int krow0, int c0, int lane) {
  const int g = lane >> 4, q = (lane & 15) >> 2, pp = lane & 3;
  v4i16_t lo, hi;
  { int r = krow0 + 8 * g + q; lo = __builtin_amdgcn_ds_read_tr16_b64_v4i16((__attribute__((address_space(3))) v4i16_t*)(sub_base + r * 128 + (((c0 + (pp >> 1)) ^ fswz(r)) * 16) + 8 * (pp & 1))); }
  { int r = krow0 + 8 * g + 4 + q; hi = __builtin_amdgcn_ds_read_tr16_b64_v4i16((__attribute__((address_space(3))) v4i16_t*)(sub_base + r * 128 + (((c0 + (pp >> 1)) ^ fswz(r)) * 16) + 8 * (pp & 1))); }
  return (bf16x8){lo[0], lo[1], lo[2], lo[3], hi[0], hi[1], hi[2], hi[3]};
}

__device__ __forceinline__ void glds16(const void* gsrc, unsigned lds_dst) {
  unsigned keep;
  asm volatile("s_mov_b32 %0, m0\n\ts_mov_b32 m0, %2\n\ts_nop 0\n\tglobal_load_lds_dwordx4 %1, off\n\ts_mov_b32 m0, %0"
               : "=&s"(keep) : "v"(gsrc), "s"(lds_dst) : "memory");
}
#define WAITV(n) asm volatile("s_waitcnt vmcnt(%0)" :: "n"(n) : "memory")
#define RAWBAR() do { asm volatile("s_waitcnt lgkmcnt(0)" ::: "memory"); __builtin_amdgcn_s_barrier(); asm volatile("" ::: "memory"); } while (0)
template <int BM, int BN, int NK, int NBUF, bool SWAP, class PT>
__device__ __forceinline__ void gemm_tile_dma(const PT& P, int m0, int n0, char* smem, f32x4 (&acc)[BM / 32][BN / 32]) {
  constexpr int ABYTES = BM * 128, BBYTES = BN * 128, STG = ABYTES + BBYTES;
  constexpr int NA = BM * 8 / 256, NB = BN * 8 / 256, NOP = NA + NB;
  const int tid = otid(), lane = tid & 63, wid = tid >> 6, wm = wid >> 1, wn = wid & 1;
  const u16* asrc[NA];
  const u16* bsrc[NB];
#pragma unroll
  for (int it = 0; it < NA; ++it) { int p = it * 256 + tid; int row = p >> 3, slot = p & 7; asrc[it] = P.a_row(m0 + row) + ((slot ^ (row & 7)) * 8); }
#pragma unroll
  for (int it = 0; it < NB; ++it) { int p = it * 256 + tid; int sub = p >> 9, r = (p >> 3) & 63, slot = p & 7; bsrc[it] = P.b_col(n0, sub) + (size_t)r * P.ldb + ((slot ^ fswz(r)) * 8); }
  const unsigned ldsw = __builtin_amdgcn_readfirstlane((unsigned)(size_t)smem + (tid >> 6) * 1024);
  auto stage_op = [&](int kt, int op) {
    const unsigned base = ldsw + (kt % NBUF) * STG;
    if (op < NA) glds16(asrc[op] + kt * 64, base + op * 4096);
    else glds16(bsrc[op - NA] + (size_t)kt * 64 * P.ldb, base + ABYTES + (op - NA) * 4096);
  };
#pragma unroll
  for (int i = 0; i < BM / 32; ++i)
#pragma unroll
    for (int j = 0; j < BN / 32; ++j) acc[i][j] = (f32x4){0.f, 0.f, 0.f, 0.f};
  const int fr = lane & 15, g = lane >> 4;
#pragma unroll
  for (int d = 0; d < NBUF - 1; ++d)
#pragma unroll
    for (int op = 0; op < NOP; ++op) stage_op(d, op);
  constexpr int NI = BM / 32, NJ = BN / 32, NM = 2 * NI * NJ;
  constexpr int EVERY = NM / NOP;
#pragma unroll
  for (int kt = 0; kt < NK; ++kt) {
    const int after = (kt + NBUF - 2 < NK - 1 ? NBUF - 2 : NK - 1 - kt);
    if (after == 0) WAITV(0); else if (after == 1) WAITV(NOP); else if (after == 2) WAITV(2 * NOP); else if (after == 3) WAITV(3 * NOP); else if (after == 4) WAITV(4 * NOP); else if (after == 5) WAITV(5 * NOP); else WAITV(6 * NOP);
    RAWBAR();
    char* base = smem + (kt % NBUF) * STG;
    char* bbase = base + ABYTES;
    bf16x8 af[2][NI], bf[2][NJ];
#pragma unroll
    for (int kk = 0; kk < 2; ++kk) {
#pragma unroll
      for (int j = 0; j < NJ; ++j) {
        int col = wn * (BN / 2) + j * 16;
        bf[kk][j] = tr_frag(bbase + (col >> 6) * 8192, kk * 32, (col & 63) >> 3, lane);
      }
#pragma unroll
      for (int i = 0; i < NI; ++i) {
        int row = wm * (BM / 2) + i * 16 + fr;
        af[kk][i] = *(const bf16x8*)(base + row * 128 + (((kk * 4 + g) ^ (row & 7)) * 16));
      }
    }
    __builtin_amdgcn_sched_barrier(0);
    if (kt + NBUF - 1 < NK) stage_op(kt + NBUF - 1, 0);
#pragma unroll
    for (int m = 0; m < NM; ++m) {
      const int kk = m / (NI * NJ), i = (m / NJ) % NI, j = m % NJ;
      acc[i][j] = SWAP ? __builtin_amdgcn_mfma_f32_16x16x32_bf16(bf[kk][j], af[kk][i], acc[i][j], 0, 0, 0)
                       : __builtin_amdgcn_mfma_f32_16x16x32_bf16(af[kk][i], bf[kk][j], acc[i][j], 0, 0, 0);
      if ((m + 1) % EVERY == 0 && (m + 1) / EVERY < NOP) {
        __builtin_amdgcn_sched_barrier(0);
        if (kt + NBUF - 1 < NK) stage_op(kt + NBUF - 1, (m + 1) / EVERY);
        __builtin_amdgcn_sched_barrier(0);
      }
    }
    __builtin_amdgcn_sched_barrier(0);
  }
  RAWBAR();
}

struct PGen {
  const u16* A; const u16* W; const u16* W2; int lda; int K; int ldb;
  __device__ const u16* a_row(int m) const { return A + (size_t)m * lda; }
  __device__ const u16* b_col(int n0, int sub) const { return W2 ? (sub ? W2 : W) + n0 : W + n0 + sub * 64; }
};

__global__ void __launch_bounds__(256) k_pre(const u16* __restrict__ c, const u16* __restrict__ wmod, const u16* __restrict__ mem0, char* ws) {
  __shared__ float red[16][17];
  __shared__ float sv[D_];
  const int b = blockIdx.x, tid = threadIdx.x;
  for (int k = tid; k < D_; k += 256) { float cv = bf2f(c[k]); sv[k] = rbf(cv / (1.f + __expf(-cv))); }
  if (b == 0) {
    u16* memb = (u16*)(ws + WS_MEMB);
    for (int gid = tid; gid < 640; gid += 256) { int u = gid >> 3, i = gid & 7; *(v4u*)(memb + gid * 8) = *(const v4u*)(mem0 + i * D_ + u * 8); }
  }
  {
    const v4u z = {0u, 0u, 0u, 0u}, f = {0xFFFFFFFFu, 0xFFFFFFFFu, 0xFFFFFFFFu, 0xFFFFFFFFu};
    for (size_t o = (size_t)(b * 256 + tid) * 16; o < CTL_BYTES; o += (size_t)240 * 256 * 16) *(v4u*)(ws + WS_CTL + o) = z;
    for (size_t o = (size_t)(b * 256 + tid) * 16; o < WS_SENT_END - WS_MEMB - 10240; o += (size_t)240 * 256 * 16) *(v4u*)(ws + WS_MEMB + 10240 + o) = f;
  }
  __syncthreads();
  const int col = tid & 15, kg = tid >> 4, n = b * 16 + col;
  float acc = 0.f;
#pragma unroll
  for (int k = 0; k < 40; ++k) acc += sv[kg * 40 + k] * bf2f(wmod[(size_t)(kg * 40 + k) * 3840 + n]);
  red[kg][col] = acc;
  __syncthreads();
  if (tid < 16) {
    float t = 0.f;
#pragma unroll
    for (int q = 0; q < 16; ++q) t += red[q][tid];
    ((float*)(ws + WS_MOD))[b * 16 + tid] = rbf(t);
  }
}

struct MegaArgs {
  const u16* x; const u16* Wq; const u16* Wk; const u16* Wv; const u16* Wo; const u16* w1; const u16* w3; const u16* w2;
  char* ws; u16* out;
};
constexpr int MEGA_LDS = 160 * 1024;
#define USLEEP 8
#define STAMP(id)
#define TSTAMP(t, id)
#define MFMA_AA(acc, wa, mb) asm volatile("v_mfma_f32_16x16x32_bf16 %0, %1, %2, %0" : "+v"(acc) : "a"(wa), "v"(mb))
#define MFMA_VV(acc, wa, mb) asm volatile("v_mfma_f32_16x16x32_bf16 %0, %1, %2, %0" : "+v"(acc) : "v"(wa), "v"(mb))
#define MFMA_BA(acc, ma, wb) asm volatile("v_mfma_f32_16x16x32_bf16 %0, %1, %2, %0" : "+v"(acc) : "v"(ma), "a"(wb))
#define MFMA_ACCA(acc, ma, wb) asm("v_mfma_f32_16x16x32_bf16 %0, %1, %2, %0" : "+a"(acc) : "v"(ma), "v"(wb))
#define MFMA_ACCV(acc, ma, wb) asm("v_mfma_f32_16x16x32_bf16 %0, %1, %2, %0" : "+v"(acc) : "v"(ma), "v"(wb))
#define MFMA16(acc, ma, wb) asm volatile("v_mfma_f32_16x16x16_bf16 %0, %1, %2, %0" : "+v"(acc) : "v"(ma), "v"(wb))

#define THROTTLE 0
#define PRE_NUM 3
__device__ __forceinline__ void presleep(unsigned long long t0, unsigned pre) {
  while ((unsigned)(__builtin_amdgcn_s_memtime() - t0) < pre) __builtin_amdgcn_s_sleep(2);
}
__device__ __forceinline__ unsigned nextpre(unsigned long long t0, unsigned& wmin) {
  unsigned wdur = (unsigned)(__builtin_amdgcn_s_memtime() - t0);
  wmin = min(wmin, wdur);
  return PRE_NUM * (wmin >> 3);
}
__device__ __forceinline__ float xmax8(float x) { return fmaxf(x, __builtin_bit_cast(float, __builtin_amdgcn_update_dpp(0, __builtin_bit_cast(int, x), 0x128, 0xf, 0xf, false))); }
__device__ __forceinline__ float xadd8(float x) { return x + __builtin_bit_cast(float, __builtin_amdgcn_update_dpp(0, __builtin_bit_cast(int, x), 0x128, 0xf, 0xf, false)); }
template <int N> __device__ __forceinline__ float dpp_ror_add(float x) { return x + __builtin_bit_cast(float, __builtin_amdgcn_update_dpp(0, __builtin_bit_cast(int, x), 0x120 + N, 0xf, 0xf, false)); }
__device__ __forceinline__ void swap16(float x, float& a, float& b) { auto r = __builtin_amdgcn_permlane16_swap(__float_as_uint(x), __float_as_uint(x), false, false); unsigned r0 = r[0], r1 = r[1]; a = __uint_as_float(r0); b = __uint_as_float(r1); }
__device__ __forceinline__ void swap32(float x, float& a, float& b) { auto r = __builtin_amdgcn_permlane32_swap(__float_as_uint(x), __float_as_uint(x), false, false); unsigned r0 = r[0], r1 = r[1]; a = __uint_as_float(r0); b = __uint_as_float(r1); }
__device__ __forceinline__ float xmax16(float x) { float a, b; swap16(x, a, b); return fmaxf(a, b); }
__device__ __forceinline__ float xadd16(float x) { float a, b; swap16(x, a, b); return a + b; }
__device__ __forceinline__ float xmax32(float x) { float a, b; swap32(x, a, b); return fmaxf(a, b); }
__device__ __forceinline__ float xadd32(float x) { float a, b; swap32(x, a, b); return a + b; }
__device__ __forceinline__ float wave_sum(float x) { x = dpp_ror_add<8>(x); x = dpp_ror_add<4>(x); x = dpp_ror_add<2>(x); x = dpp_ror_add<1>(x); return xadd32(xadd16(x)); }

__device__ __forceinline__ void wo_op_(const MegaArgs& a, char* smem, int tid, int h, int pass, int it) {
  const u16* wosrc = a.Wo + (size_t)h * DH_ * D_ + (size_t)(tid >> 3) * D_ + (((tid & 7) ^ fswz(tid >> 3)) * 8);
  const unsigned ldsw = __builtin_amdgcn_readfirstlane((unsigned)(size_t)smem + (tid >> 6) * 1024);
  glds16(wosrc + (size_t)it * 32 * D_ + pass * 64, ldsw + (pass & 3) * 24576 + it * 4096);
}
template <int NST0, int NST1>
__device__ __forceinline__ void u_gemm(const MegaArgs& a, char* smem, const int tid, const int urow0, const int h) {
  const int lane = tid & 63, wid = tid >> 6, wm = wid >> 1, wn = wid & 1, fr = lane & 15, g = lane >> 4;
  char* vimg = smem + 96 * 1024;
  const __amdgpu_buffer_rsrc_t ru = __builtin_amdgcn_make_buffer_rsrc(a.ws + WS_U, 0, H_ * UCH_ * D_ * 16, 0x00020000);
#pragma unroll
  for (int pass = 0; pass < 10; ++pass) {
    constexpr int N0[10] = {12, 16, 20, 24, 24, 24, 24, 24, 18, 12};
    if (pass < 3 && NST0 != NST1) { if (wn == 0) WAITV(N0[pass] + NST0); else WAITV(N0[pass] + NST1); } else if (pass < 3) WAITV(N0[pass] + NST0); else WAITV(N0[pass]);
    RAWBAR();
    char* bb = smem + (pass & 3) * 24576;
    f32x4 acc[4][2];
#pragma unroll
    for (int i = 0; i < 4; ++i) { acc[i][0] = (f32x4){0.f, 0.f, 0.f, 0.f}; acc[i][1] = (f32x4){0.f, 0.f, 0.f, 0.f}; }
    bf16x8 af[2][4], bf[2][2];
    auto rdfr = [&](int ks, int sl) {
      const int kt = ks >> 1, kk = ks & 1;
#pragma unroll
      for (int j = 0; j < 2; ++j) bf[sl][j] = tr_frag(bb + kt * 8192, kk * 32, (wn * 32 + j * 16) >> 3, lane);
#pragma unroll
      for (int i = 0; i < 4; ++i) { int row = wm * 64 + i * 16 + fr; af[sl][i] = *(const bf16x8*)(vimg + kt * 16384 + row * 128 + (((kk * 4 + g) ^ (row & 7)) * 16)); }
    };
    rdfr(0, 0);
#pragma unroll
    for (int ks = 0; ks < 6; ++ks) {
      if (ks + 1 < 6) rdfr(ks + 1, (ks + 1) & 1);
      if (pass + 3 < 10) wo_op_(a, smem, tid, h, pass + 3, ks);
      __builtin_amdgcn_sched_barrier(0);
#pragma unroll
      for (int i = 0; i < 4; ++i)
#pragma unroll
        for (int j = 0; j < 2; ++j) acc[i][j] = __builtin_amdgcn_mfma_f32_16x16x32_bf16(af[ks & 1][i], bf[ks & 1][j], acc[i][j], 0, 0, 0);
      __builtin_amdgcn_sched_barrier(0);
    }
#pragma unroll
    for (int i = 0; i < 4; i += 2)
#pragma unroll
      for (int j = 0; j < 2; ++j) {
        v4u v = pair16(pk4(acc[i][j]), pk4(acc[i + 1][j]));
        int row = urow0 + wm * 64 + (i + (g & 1)) * 16 + (g >> 1) * 8, c = pass * 64 + wn * 32 + j * 16 + fr;
        __builtin_amdgcn_raw_buffer_store_b128(v, ru, ((h * UCH_ + (row >> 3)) * D_ + c) * 16, 0, 16);
      }
  }
}

__device__ __forceinline__ void task_b(const MegaArgs& a, char* smem, const int q) {
  const int mb = q % 5, hh = q / 5;
  const int t_ = otid();
  const unsigned ldsw = __builtin_amdgcn_readfirstlane((unsigned)(size_t)smem + (t_ >> 6) * 1024);
#pragma unroll
  for (int it = 0; it < 12; ++it) {
    const int row = (it & 3) * 32 + (t_ >> 3);
    glds16(a.Wv + (size_t)(mb * 128 + row) * HD_ + hh * DH_ + (it >> 2) * 64 + (((t_ & 7) ^ (row & 7)) * 8), ldsw + 96 * 1024 + it * 4096);
  }
#pragma unroll
  for (int ps = 0; ps < 3; ++ps)
#pragma unroll
    for (int it = 0; it < 6; ++it) wo_op_(a, smem, t_, hh, ps, it);
  u_gemm<0, 0>(a, smem, t_, S_ + mb * 128, hh);
  publish(ctl(a.ws, C_B));
}

__device__ __forceinline__ void score_role(const MegaArgs& a, char* smem, const int b) {
  const int tid = threadIdx.x, lane = tid & 63, w = __builtin_amdgcn_readfirstlane(tid >> 6);
  const int fr = lane & 15, g = lane >> 4;
  const __amdgpu_buffer_rsrc_t rm = __builtin_amdgcn_make_buffer_rsrc(a.ws + WS_MEMB, 0, 65 * 10240, 0x00020000);
  const __amdgpu_buffer_rsrc_t rp = __builtin_amdgcn_make_buffer_rsrc(a.ws + WS_PB, 0, 64 * 20480, 0x00020000);
  const __amdgpu_buffer_rsrc_t rkm = __builtin_amdgcn_make_buffer_rsrc(a.ws + WS_KM, 0, NSEG_ * 8 * HD_ * 2, 0x00020000);
  const u16* Kx = (const u16*)(a.ws + WS_K);
  unsigned* tmo = ctl(a.ws, C_TMO);
  const int moff = (g * 8 + (fr & 7)) * 16;
  const int h = b;
  char* lw = smem;
  float* part = (float*)(smem + 144 * 1024);
  task_b(a, smem, NU_ + h);
  bf16x8 wa[64], wv[20];
  {
    const unsigned ldsw = __builtin_amdgcn_readfirstlane((unsigned)(size_t)smem + w * 1024);
    const size_t boff = (size_t)(tid >> 3) * HD_ + h * DH_ + (((tid & 7) ^ fswz(tid >> 3)) * 8);
    auto stage = [&](int ks) {
#pragma unroll
      for (int it = 0; it < 6; ++it) glds16((it < 3 ? a.Wq : a.Wk) + boff + (size_t)(ks * 32) * HD_ + (it % 3) * 64, ldsw + (ks & 3) * 24576 + it * 4096);
    };
    bf16x8* scr = (bf16x8*)(a.ws + WS_WT) + ((size_t)(h * 4 + w) * 36) * 64 + lane;
    stage(0); stage(1); stage(2);
#pragma unroll
    for (int ks = 0; ks < 20; ++ks) {
      if (ks < 18) WAITV(12); else if (ks == 18) WAITV(6); else WAITV(0);
      RAWBAR();
      if (ks + 3 < 20) {
        stage(ks + 3);
      }
#pragma unroll
      for (int p = 0; p < 6; ++p) {
        const int etg = 3 * w + (p % 3);
        bf16x8 v = tr_frag(smem + (ks & 3) * 24576 + (p / 3) * 12288 + (etg >> 2) * 4096, 0, (etg & 3) * 2, lane);
        const int f = p * 20 + ks;
        if (f < 64) wa[f] = v; else if (f < 84) wv[f - 64] = v; else scr[(f - 84) * 64] = v;
      }
    }
    asm volatile("s_waitcnt vmcnt(0)" ::: "memory");
    __syncthreads();
    for (int f = 0; f < 36; ++f) *(bf16x8*)(lw + (w * 36 + f) * 1024 + lane * 16) = scr[f * 64];
  }
  const char* lwp = lw + w * 36 * 1024 + lane * 16;
  const float scale = 0.07216878364870322f;
  unsigned pre_m = 0, wmin_m = 0xFFFFFFFFu;
  for (int s = 0; s < NSEG_; ++s) {
    if ((s & 3) == 0) {
      if (tid == 0) { spin_ge(ctl(a.ws, C_K + (s >> 2)), s < 8 ? 64u : 32u, tmo); acquire_all(); }
      __syncthreads();
    }
    STAMP(0);
    bf16x4 kx[2][3];
#pragma unroll
    for (int kt = 0; kt < 2; ++kt)
#pragma unroll
      for (int et = 0; et < 3; ++et)
        kx[kt][et] = *(const bf16x4*)(Kx + (size_t)(s * 32 + kt * 16 + fr) * HD_ + h * DH_ + (3 * w + et) * 16 + 4 * g);
    bf16x8 l5[2];
    l5[0] = *(const bf16x8*)(lwp + 16 * 1024);
    l5[1] = *(const bf16x8*)(lwp + 17 * 1024);
    v4u mv[20];
    {
      unsigned spins = 0;
      const unsigned long long t0 = __builtin_amdgcn_s_memtime();
      presleep(t0, pre_m);
      for (;;) {
        unsigned mx = 0;
#pragma unroll
        for (int k = 0; k < 20; ++k) mv[k] = __builtin_amdgcn_raw_buffer_load_b128(rm, s * 10240 + k * 512 + moff, 0, 16);
#pragma unroll
        for (int k = 0; k < 20; ++k) mx = umax4(mx, mv[k]);
        if (!__any(mx == 0xFFFFFFFFu)) break;
        if (++spins > SPIN_CAP) { __hip_atomic_store(tmo, 1u, __ATOMIC_RELAXED, __HIP_MEMORY_SCOPE_AGENT); break; }
      }
      pre_m = nextpre(t0, wmin_m);
    }
    STAMP(1);
    f32x4 acc[6];
#pragma unroll
    for (int p = 0; p < 6; ++p) acc[p] = (f32x4){0.f, 0.f, 0.f, 0.f};
    bf16x8 l4[2];
    l4[0] = *(const bf16x8*)(lwp + 0 * 1024);
    l4[1] = *(const bf16x8*)(lwp + 1 * 1024);
    asm volatile("s_nop 1");
#pragma unroll
    for (int ks = 0; ks < 20; ++ks) {
      const bf16x8 mf = __builtin_bit_cast(bf16x8, mv[ks]);
      MFMA_AA(acc[0], wa[ks], mf);
      MFMA_AA(acc[1], wa[20 + ks], mf);
      MFMA_AA(acc[2], wa[40 + ks], mf);
      if (ks < 4) MFMA_AA(acc[3], wa[60 + ks], mf); else MFMA_VV(acc[3], wv[ks - 4], mf);
      if (ks < 4) MFMA_VV(acc[4], wv[16 + ks], mf);
      else { MFMA_VV(acc[4], l4[ks & 1], mf); if (ks + 2 < 20) l4[ks & 1] = *(const bf16x8*)(lwp + (ks + 2 - 4) * 1024); }
      MFMA_VV(acc[5], l5[ks & 1], mf);
      if (ks + 2 < 20) l5[ks & 1] = *(const bf16x8*)(lwp + (16 + ks + 2) * 1024);
    }
    asm volatile("s_nop 7\n\ts_nop 1" : "+v"(acc[0]), "+v"(acc[1]), "+v"(acc[2]), "+v"(acc[3]), "+v"(acc[4]), "+v"(acc[5]));
    STAMP(3);
    bf16x4 qf[3], kf[3];
#pragma unroll
    for (int et = 0; et < 3; ++et) { qf[et] = __builtin_bit_cast(bf16x4, pk4(acc[et])); kf[et] = __builtin_bit_cast(bf16x4, pk4(acc[3 + et])); }
    f32x4 st[3];
#pragma unroll
    for (int kt = 0; kt < 3; ++kt) st[kt] = (f32x4){0.f, 0.f, 0.f, 0.f};
    asm volatile("s_nop 1" : "+v"(qf[0]), "+v"(qf[1]), "+v"(qf[2]), "+v"(kf[0]), "+v"(kf[1]), "+v"(kf[2]), "+v"(st[0]), "+v"(st[1]), "+v"(st[2]));
#pragma unroll
    for (int et = 0; et < 3; ++et) {
      MFMA16(st[0], kf[et], qf[et]);
      MFMA16(st[1], kx[0][et], qf[et]);
      MFMA16(st[2], kx[1][et], qf[et]);
    }
    asm volatile("s_nop 7\n\ts_nop 1" : "+v"(st[0]), "+v"(st[1]), "+v"(st[2]));
    float* pt = part + (s & 1) * 1280;
    if (fr < 8) {
      if (g < 2) *(f32x4*)(pt + (w * 8 + fr) * 40 + 4 * g) = st[0];
      *(f32x4*)(pt + (w * 8 + fr) * 40 + 8 + 4 * g) = st[1];
      *(f32x4*)(pt + (w * 8 + fr) * 40 + 24 + 4 * g) = st[2];
    }
    STAMP(4);
    __syncthreads();
    STAMP(5);
    if (w == 0) {
      const int i = lane & 7, jc = lane >> 3;
      const int jr = jc < 5 ? jc : 0;
      float sc[8];
      f32x4 pp[8];
#pragma unroll
      for (int ww = 0; ww < 4; ++ww) { pp[2 * ww] = *(const f32x4*)(pt + (ww * 8 + i) * 40 + jr * 8); pp[2 * ww + 1] = *(const f32x4*)(pt + (ww * 8 + i) * 40 + jr * 8 + 4); }
      __builtin_amdgcn_sched_barrier(0);
#pragma unroll
      for (int e = 0; e < 4; ++e) { sc[e] = (pp[0][e] + pp[2][e]) + (pp[4][e] + pp[6][e]); sc[4 + e] = (pp[1][e] + pp[3][e]) + (pp[5][e] + pp[7][e]); }
      float mx = -3.0e38f;
#pragma unroll
      for (int e = 0; e < 8; ++e) { sc[e] = rbf(rbf(sc[e]) * scale); mx = fmaxf(mx, sc[e]); }
      mx = xmax8(mx); mx = xmax16(mx); mx = xmax32(mx);
      float sum = 0.f;
#pragma unroll
      for (int e = 0; e < 8; ++e) { sc[e] = __expf(sc[e] - mx); sum += sc[e]; }
      if (jc >= 5) sum = 0.f;
      sum = xadd8(sum); sum = xadd16(sum); sum = xadd32(sum);
      if (jc < 5) {
        const float inv = 1.f / sum;
        v4u o = {canon2(pack2(sc[0] * inv, sc[1] * inv)), canon2(pack2(sc[2] * inv, sc[3] * inv)), canon2(pack2(sc[4] * inv, sc[5] * inv)), canon2(pack2(sc[6] * inv, sc[7] * inv))};
        const int kc = (jc == 0) ? 128 + h : h * 4 + jc - 1;
        __builtin_amdgcn_raw_buffer_store_b128(o, rp, s * 20480 + kc * 128 + i * 16, 0, 16);
      }
    } else if (tid == 64 && s > 0) {
      __hip_atomic_fetch_add(ctl(a.ws, C_S + s - 1), 1u, __ATOMIC_RELAXED, __HIP_MEMORY_SCOPE_AGENT);
    }
    if (fr < 8) {
#pragma unroll
      for (int et = 0; et < 3; ++et)
        __builtin_amdgcn_raw_buffer_store_b64(__builtin_bit_cast(v2u, kf[et]), rkm, ((s * 8 + fr) * HD_ + h * DH_ + (3 * w + et) * 16 + 4 * g) * 2, 0, 16);
    }
    STAMP(6);
    __syncthreads();
  }
  publish(ctl(a.ws, C_S + NSEG_ - 1));
}

__device__ __forceinline__ void u_role(const MegaArgs& a, char* smem, const int b) {
  const int tid = threadIdx.x, lane = tid & 63, w = __builtin_amdgcn_readfirstlane(tid >> 6);
  const int fr = lane & 15, g = lane >> 4;
  const __amdgpu_buffer_rsrc_t rm = __builtin_amdgcn_make_buffer_rsrc(a.ws + WS_MEMB, 0, 65 * 10240, 0x00020000);
  const __amdgpu_buffer_rsrc_t rp = __builtin_amdgcn_make_buffer_rsrc(a.ws + WS_PB, 0, 64 * 20480, 0x00020000);
  const __amdgpu_buffer_rsrc_t rum = __builtin_amdgcn_make_buffer_rsrc(a.ws + WS_UM, 0, NSEG_ * H_ * D_ * 16, 0x00020000);
  const u16* U = (const u16*)(a.ws + WS_U);
  unsigned* tmo = ctl(a.ws, C_TMO);
  const int moff = (g * 8 + (fr & 7)) * 16;
  const int u = b - H_;
  char* lub = smem;
  float* red = (float*)(smem + 4096);
  task_b(a, smem, u);
  if (tid == 0) { spin_ge(ctl(a.ws, C_B), 160u, tmo); acquire_all(); }
  __syncthreads();
  bf16x8 ba[64], bv[16];
#pragma unroll
  for (int f = 0; f < 80; ++f) {
    const int nt = f / 20, ks = f % 20;
    const int hh = (4 * w + nt) * 2 + (fr >> 3), c = 8 * u + (fr & 7);
    bf16x8 v = *(const bf16x8*)(U + (((size_t)hh * UCH_ + 256 + ks * 4 + g) * D_ + c) * 8);
    if (f < 64) ba[f] = v; else bv[f - 64] = v;
  }
  unsigned pre_m = 0, pre_p = 0, wmin_m = 0xFFFFFFFFu, wmin_p = 0xFFFFFFFFu;
  for (int s = 0; s < NSEG_; ++s) {
    if ((s & 3) == 0) {
      if (tid == 0) { spin_ge(ctl(a.ws, C_K + (s >> 2)), s < 8 ? 64u : 32u, tmo); acquire_all(); }
      __syncthreads();
    }
    STAMP(0);
    bf16x8 ub[10];
#pragma unroll
    for (int k = 0; k < 8; ++k) ub[k] = *(const bf16x8*)(U + (((size_t)(8 * w + k) * UCH_ + s * 4 + g) * D_ + 8 * u + (fr & 7)) * 8);
    v4u mv[20];
    {
      unsigned spins = 0;
      const unsigned long long t0 = __builtin_amdgcn_s_memtime();
      presleep(t0, pre_m);
      for (;;) {
        __builtin_amdgcn_s_sleep(USLEEP);
        unsigned mx = 0;
#pragma unroll
        for (int k = 0; k < 20; ++k) mv[k] = __builtin_amdgcn_raw_buffer_load_b128(rm, s * 10240 + k * 512 + moff, 0, 16);
#pragma unroll
        for (int k = 0; k < 20; ++k) mx = umax4(mx, mv[k]);
        if (!__any(mx == 0xFFFFFFFFu)) break;
        if (++spins > SPIN_CAP) { __hip_atomic_store(tmo, 1u, __ATOMIC_RELAXED, __HIP_MEMORY_SCOPE_AGENT); break; }
      }
      pre_m = nextpre(t0, wmin_m);
    }
    STAMP(1);
    {
      f32x4 acc[4];
#pragma unroll
      for (int nt = 0; nt < 4; ++nt) acc[nt] = (f32x4){0.f, 0.f, 0.f, 0.f};
      asm volatile("s_nop 1");
#pragma unroll
      for (int ks = 0; ks < 20; ++ks) {
        const bf16x8 mf = __builtin_bit_cast(bf16x8, mv[ks]);
        MFMA_BA(acc[0], mf, ba[ks]);
        MFMA_BA(acc[1], mf, ba[20 + ks]);
        MFMA_BA(acc[2], mf, ba[40 + ks]);
        if (ks < 4) MFMA_BA(acc[3], mf, ba[60 + ks]); else MFMA_VV(acc[3], mf, bv[ks - 4]);
      }
      asm volatile("s_nop 7\n\ts_nop 1" : "+v"(acc[0]), "+v"(acc[1]), "+v"(acc[2]), "+v"(acc[3]));
      if (g < 2) {
#pragma unroll
        for (int nt = 0; nt < 4; ++nt) {
          int hh = (4 * w + nt) * 2 + (fr >> 3), cc = fr & 7;
          v2u v = pk4(acc[nt]);
          *(v2u*)(lub + (hh * 8 + cc) * 16 + g * 8) = v;
          __builtin_amdgcn_raw_buffer_store_b64(v, rum, (((s * H_ + hh) * D_ + 8 * u + cc) * 8 + g * 4) * 2, 0, 16);
        }
      }
    }
    STAMP(2);
    __syncthreads();
    if (tid == 64 && s > 0) __hip_atomic_fetch_add(ctl(a.ws, C_S + s - 1), 1u, __ATOMIC_RELAXED, __HIP_MEMORY_SCOPE_AGENT);
#pragma unroll
    for (int k = 0; k < 2; ++k) ub[8 + k] = *(const bf16x8*)(lub + ((4 * (2 * w + k) + g) * 8 + (fr & 7)) * 16);
    STAMP(3);
    v4u pv[10];
    {
      unsigned spins = 0;
      const unsigned long long t0 = __builtin_amdgcn_s_memtime();
      presleep(t0, pre_p);
      for (;;) {
        unsigned mx = 0;
#pragma unroll
        for (int k = 0; k < 8; ++k) pv[k] = __builtin_amdgcn_raw_buffer_load_b128(rp, s * 20480 + (8 * w + k) * 512 + moff, 0, 16);
#pragma unroll
        for (int k = 0; k < 2; ++k) pv[8 + k] = __builtin_amdgcn_raw_buffer_load_b128(rp, s * 20480 + (32 + 2 * w + k) * 512 + moff, 0, 16);
#pragma unroll
        for (int k = 0; k < 10; ++k) mx = umax4(mx, pv[k]);
        if (!__any(mx == 0xFFFFFFFFu)) break;
        if (++spins > SPIN_CAP) { __hip_atomic_store(tmo, 1u, __ATOMIC_RELAXED, __HIP_MEMORY_SCOPE_AGENT); break; }
      }
      pre_p = nextpre(t0, wmin_p);
    }
    STAMP(4);
    f32x4 acc0 = {0.f, 0.f, 0.f, 0.f}, acc1 = {0.f, 0.f, 0.f, 0.f};
    asm volatile("s_nop 1" : "+v"(acc0), "+v"(acc1));
#pragma unroll
    for (int k = 0; k < 10; k += 2) {
      MFMA_VV(acc0, ub[k], __builtin_bit_cast(bf16x8, pv[k]));
      MFMA_VV(acc1, ub[k + 1], __builtin_bit_cast(bf16x8, pv[k + 1]));
    }
    asm volatile("s_nop 7\n\ts_nop 1" : "+v"(acc0), "+v"(acc1));
    acc0 += acc1;
    if (fr < 8 && g < 2) *(f32x4*)(red + (w * 8 + fr) * 8 + 4 * g) = acc0;
    __syncthreads();
    STAMP(5);
    if (tid < 16) {
      const int i = tid >> 1, hf = tid & 1;
      f32x4 o = *(const f32x4*)(red + (0 * 8 + i) * 8 + 4 * hf);
#pragma unroll
      for (int ww = 1; ww < 4; ++ww) o += *(const f32x4*)(red + (ww * 8 + i) * 8 + 4 * hf);
      v2u v = {canon2(pack2(o[0], o[1])), canon2(pack2(o[2], o[3]))};
      __builtin_amdgcn_raw_buffer_store_b64(v, rm, (s + 1) * 10240 + u * 128 + tid * 8, 0, 16);
    }
    STAMP(6);
  }
  publish(ctl(a.ws, C_S + NSEG_ - 1));
}

#define QKV_COMPUTE(BASE, KN) do { \
    char* base_ = (BASE); \
    bf16x8 af[4], bf[4]; \
    _Pragma("unroll") for (int i = 0; i < 4; ++i) { int row = wm * 64 + i * 16 + fr; af[i] = *(const bf16x8*)(base_ + row * 64 + ((g ^ (((row >> 3) & 1) * 3)) * 16)); } \
    _Pragma("unroll") for (int j = 0; j < 4; ++j) { const int nt = wn * NTW + j; bf[j] = tr_frag(base_ + 8192 + (nt >> 2) * 4096, 0, (nt & 3) * 2, lane); } \
    _Pragma("unroll") for (int j = 0; j < NTW; ++j) { \
      __builtin_amdgcn_sched_barrier(0); \
      _Pragma("unroll") for (int i = 0; i < 4; ++i) { if (i < 3 || j < 10) MFMA_ACCA(acc[i][j], bf[j & 3], af[i]); else MFMA_ACCV(acc[i][j], bf[j & 3], af[i]); } \
      __builtin_amdgcn_sched_barrier(0); \
      if (j + 4 < NTW) { const int nt = wn * NTW + j + 4; bf[j & 3] = tr_frag(base_ + 8192 + (nt >> 2) * 4096, 0, (nt & 3) * 2, lane); } \
      if ((KN) >= 0) { const int o0 = (j * NOPS + NTW - 1) / NTW, o1 = ((j + 1) * NOPS + NTW - 1) / NTW; if (o1 > o0 && o0 < NOPS) stage_op((KN), o0); } \
    } } while (0)
template <int SUB0, int NS>
__device__ __forceinline__ void task_qkvu(const MegaArgs& a, char* smem, int mt, int h) {
  constexpr int BM = 128, NTW = 2 * NS, NOPS = 2 + NS;
  constexpr bool HASV = (SUB0 + NS == 9);
  const int tid = otid(), lane = tid & 63, wid = tid >> 6, wm = wid >> 1, wn = wid & 1, fr = lane & 15, g = lane >> 4;
  char* vimg = smem + 96 * 1024;
  const unsigned ldsw = __builtin_amdgcn_readfirstlane((unsigned)(size_t)smem + (tid >> 6) * 1024);
  auto wo_stage = [&](int pass) {
#pragma unroll
    for (int it = 0; it < 6; ++it) wo_op_(a, smem, tid, h, pass, it);
  };
  {
    constexpr int STG = 8192 + NS * 4096;
    const u16* asrc = (const u16*)(a.ws + WS_H1) + (size_t)(mt * BM + (tid >> 2)) * D_ + (((tid & 3) ^ (((tid >> 5) & 1) * 3)) * 8);
    const size_t boff = (size_t)(tid >> 3) * HD_ + h * DH_ + (((tid & 7) ^ fswz(tid >> 3)) * 8);
    auto stage_op = [&](int kt, int op) {
      const unsigned base = ldsw + (kt % 3) * STG;
      if (op < 2) glds16(asrc + (size_t)op * 64 * D_ + kt * 32, base + op * 4096);
      else {
        const int it = op - 2, sb = SUB0 + it;
        const u16* W = (sb < 3) ? a.Wq : (sb < 6 ? a.Wk : a.Wv);
        glds16(W + boff + (size_t)(kt * 32) * HD_ + (sb % 3) * 64, base + 8192 + it * 4096);
      }
    };
    f32x4 acc[4][NTW];
#pragma unroll
    for (int i = 0; i < 4; ++i)
#pragma unroll
      for (int j = 0; j < NTW; ++j) acc[i][j] = (f32x4){0.f, 0.f, 0.f, 0.f};
#pragma unroll
    for (int op = 0; op < NOPS; ++op) stage_op(0, op);
#pragma unroll
    for (int op = 0; op < NOPS; ++op) stage_op(1, op);
    for (int kb = 0; kb < 18; kb += 3) {
#pragma unroll
      for (int d = 0; d < 3; ++d) {
        const int kt = kb + d;
        WAITV(NOPS);
        RAWBAR();
        QKV_COMPUTE(smem + d * STG, kt + 2);
      }
    }
    WAITV(NOPS); RAWBAR(); QKV_COMPUTE(smem + 0 * STG, -1);
    WAITV(0); RAWBAR(); QKV_COMPUTE(smem + 1 * STG, -1);
    RAWBAR();
#pragma unroll
    for (int j = 0; j < NTW; ++j) asm volatile("s_nop 7\n\ts_nop 1" : "+a"(acc[0][j]), "+a"(acc[1][j]), "+a"(acc[2][j]));
#pragma unroll
    for (int j = 0; j < NTW; ++j) { if (j < 10) asm volatile("s_nop 7\n\ts_nop 1" : "+a"(acc[3][j])); else asm volatile("s_nop 7\n\ts_nop 1" : "+v"(acc[3][j])); }
    TSTAMP(mt * 32 + h, 3);
    if (HASV) { wo_stage(0); wo_stage(1); wo_stage(2); }
    const __amdgpu_buffer_rsrc_t rq = __builtin_amdgcn_make_buffer_rsrc(a.ws + WS_Q, 0, 2 * S_ * HD_ * 2, 0x00020000);
#pragma unroll
    for (int j = 0; j < NTW; j += 2) {
      const int nt = SUB0 * 4 + wn * NTW + j;
      const int mat = nt / 12, nl = nt - mat * 12;
#pragma unroll
      for (int i = 0; i < 4; ++i) {
        if (mat < 2) {
          v4u v = pair16(pk4(acc[i][j]), pk4(acc[i][j + 1]));
          int tok = mt * BM + wm * 64 + i * 16 + fr, n = h * DH_ + (nl + (g & 1)) * 16 + (g >> 1) * 8;
          __builtin_amdgcn_raw_buffer_store_b128(v, rq, mat * (S_ * HD_ * 2) + (tok * HD_ + n) * 2, 0, 16);
        } else if (HASV) {
#pragma unroll
          for (int jj = 0; jj < 2; ++jj) {
            int row = wm * 64 + i * 16 + fr, n = (nl + jj) * 16 + 4 * g;
            *(v2u*)(vimg + (n >> 6) * 16384 + row * 128 + ((((n >> 3) & 7) ^ (row & 7)) * 16) + ((n >> 2) & 1) * 8) = pk4(acc[i][j + jj]);
          }
        }
      }
    }
  }
  if (HASV) u_gemm<(NS == 9 ? 36 : 0), (NS == 9 ? 12 : 0)>(a, smem, tid, mt * BM, h);
}

__device__ __forceinline__ void task_x(const MegaArgs& a, char* smem, int s, int hq) {
  const int tid = otid(), lane = tid & 63, w = tid >> 6, fr = lane & 15, g = lane >> 4;
  const u16* Q = (const u16*)(a.ws + WS_Q);
  const u16* Kx = (const u16*)(a.ws + WS_K);
  const u16* km = (const u16*)(a.ws + WS_KM);
  const u16* U = (const u16*)(a.ws + WS_U);
  const u16* Um = (const u16*)(a.ws + WS_UM);
  constexpr int PSTR = 336;
  const float scale = 0.07216878364870322f;
  bf16x8 uf[5][10];
  bf16x8 kfa[6][3], qfa[6][2];
#pragma unroll
  for (int ks = 0; ks < 6; ++ks) {
    const int eo = (4 * hq + w) * DH_ + ks * 32 + 8 * g;
    kfa[ks][0] = *(const bf16x8*)(km + ((size_t)s * 8 + (fr & 7)) * HD_ + eo);
    kfa[ks][1] = *(const bf16x8*)(Kx + (size_t)(s * 32 + fr) * HD_ + eo);
    kfa[ks][2] = *(const bf16x8*)(Kx + (size_t)(s * 32 + 16 + fr) * HD_ + eo);
    qfa[ks][0] = *(const bf16x8*)(Q + (size_t)(s * 32 + fr) * HD_ + eo);
    qfa[ks][1] = *(const bf16x8*)(Q + (size_t)(s * 32 + 16 + fr) * HD_ + eo);
  }
  auto uload = [&](int ks) {
    const int kc = ks * 4 + g, hl = kc / 5, jc = kc - hl * 5, h = 4 * hq + hl;
    const u16* ub = ((jc == 0) ? Um + (((size_t)s * H_ + h) * D_) * 8 : U + (((size_t)h * UCH_ + s * 4 + jc - 1) * D_) * 8) + (size_t)(160 * w + fr) * 8;
#pragma unroll
    for (int ct = 0; ct < 10; ++ct) uf[ks][ct] = *(const bf16x8*)(ub + ct * 128);
  };
  uload(0); uload(1);
  __builtin_amdgcn_sched_barrier(0);
  {
    f32x4 acc[3][2];
#pragma unroll
    for (int kt = 0; kt < 3; ++kt) { acc[kt][0] = (f32x4){0.f, 0.f, 0.f, 0.f}; acc[kt][1] = (f32x4){0.f, 0.f, 0.f, 0.f}; }
#pragma unroll
    for (int ks = 0; ks < 6; ++ks)
#pragma unroll
      for (int kt = 0; kt < 3; ++kt)
#pragma unroll
        for (int tt = 0; tt < 2; ++tt) acc[kt][tt] = __builtin_amdgcn_mfma_f32_16x16x32_bf16(kfa[ks][kt], qfa[ks][tt], acc[kt][tt], 0, 0, 0);
    __builtin_amdgcn_sched_barrier(0);
    uload(2); uload(3); uload(4);
    __builtin_amdgcn_sched_barrier(0);
#pragma unroll
    for (int tt = 0; tt < 2; ++tt) {
      const int t = tt * 16 + fr;
      float sc[12]; bool ok[12];
      float mx = -3.0e38f;
#pragma unroll
      for (int kt = 0; kt < 3; ++kt)
#pragma unroll
        for (int r = 0; r < 4; ++r) {
          int jl = 4 * g + r;
          bool v = (kt == 0) ? (jl < 8) : ((kt - 1) * 16 + jl <= t);
          float xx = rbf(rbf(acc[kt][tt][r]) * scale);
          sc[kt * 4 + r] = xx; ok[kt * 4 + r] = v;
          if (v) mx = fmaxf(mx, xx);
        }
      mx = xmax16(mx); mx = xmax32(mx);
      float sum = 0.f;
#pragma unroll
      for (int e = 0; e < 12; ++e) { sc[e] = ok[e] ? __expf(sc[e] - mx) : 0.f; sum += sc[e]; }
      sum = xadd16(sum); sum = xadd32(sum);
      const float inv = 1.f / sum;
      char* dst = smem + t * PSTR + w * 80;
      if (g < 2) *(v2u*)(dst + 8 * g) = (v2u){pack2(sc[0] * inv, sc[1] * inv), pack2(sc[2] * inv, sc[3] * inv)};
      *(v2u*)(dst + 16 + 8 * g) = (v2u){pack2(sc[4] * inv, sc[5] * inv), pack2(sc[6] * inv, sc[7] * inv)};
      *(v2u*)(dst + 48 + 8 * g) = (v2u){pack2(sc[8] * inv, sc[9] * inv), pack2(sc[10] * inv, sc[11] * inv)};
    }
  }
  __syncthreads();
  f32x4 acc[10][2];
#pragma unroll
  for (int ct = 0; ct < 10; ++ct) { acc[ct][0] = (f32x4){0.f, 0.f, 0.f, 0.f}; acc[ct][1] = (f32x4){0.f, 0.f, 0.f, 0.f}; }
#pragma unroll
  for (int ks = 0; ks < 5; ++ks) {
    bf16x8 pf[2];
    pf[0] = *(const bf16x8*)(smem + fr * PSTR + (ks * 4 + g) * 16);
    pf[1] = *(const bf16x8*)(smem + (16 + fr) * PSTR + (ks * 4 + g) * 16);
#pragma unroll
    for (int ct = 0; ct < 10; ++ct)
#pragma unroll
      for (int tt = 0; tt < 2; ++tt) acc[ct][tt] = __builtin_amdgcn_mfma_f32_16x16x32_bf16(uf[ks][ct], pf[tt], acc[ct][tt], 0, 0, 0);
  }
  const __amdgpu_buffer_rsrc_t rx = __builtin_amdgcn_make_buffer_rsrc(a.ws + WS_XP, 0, NSEG_ * 8 * 32 * D_ * 4, 0x00020000);
#pragma unroll
  for (int ct = 0; ct < 10; ++ct)
#pragma unroll
    for (int tt = 0; tt < 2; ++tt) {
      const int t = tt * 16 + fr, c = 160 * w + ct * 16 + 4 * g;
      __builtin_amdgcn_raw_buffer_store_b128(__builtin_bit_cast(v4u, acc[ct][tt]), rx, (((s * 8 + hq) * 32 + t) * D_ + c) * 4, 0, 16);
    }
}

__device__ __forceinline__ void task_xc(const MegaArgs& a, char* smem, int s, int q8) {
  const int tid = otid(), lane = tid & 63, w = tid >> 6;
  const float* mod = (const float*)(a.ws + WS_MOD);
  const float* xp = (const float*)(a.ws + WS_XP) + (size_t)s * 8 * 32 * D_;
  const __amdgpu_buffer_rsrc_t rx1 = __builtin_amdgcn_make_buffer_rsrc(a.ws + WS_X1, 0, S_ * D_ * 2, 0x00020000);
  const __amdgpu_buffer_rsrc_t rh2 = __builtin_amdgcn_make_buffer_rsrc(a.ws + WS_H2, 0, S_ * D_ * 2, 0x00020000);
  float* xl = (float*)smem + w * D_;
  const int t0 = 4 * q8 + w;
  const bool two = lane < 16;
  const int c1 = two ? 512 + lane * 8 : lane * 8;
  f32x4 msb[2][2], mcb[2][2];
  {
    f32x4 pp[2][8][2]; v4u xx[2]; f32x4 gg[2][2];
#pragma unroll
    for (int k = 0; k < 2; ++k) {
      const int c0 = k ? c1 : lane * 8;
#pragma unroll
      for (int pq = 0; pq < 8; ++pq) { pp[k][pq][0] = *(const f32x4*)(xp + ((size_t)pq * 32 + t0) * D_ + c0); pp[k][pq][1] = *(const f32x4*)(xp + ((size_t)pq * 32 + t0) * D_ + c0 + 4); }
      xx[k] = *(const v4u*)(a.x + (size_t)(s * 32 + t0) * D_ + c0);
      gg[k][0] = *(const f32x4*)(mod + 2 * D_ + c0); gg[k][1] = *(const f32x4*)(mod + 2 * D_ + c0 + 4);
      msb[k][0] = *(const f32x4*)(mod + 3 * D_ + c0); msb[k][1] = *(const f32x4*)(mod + 3 * D_ + c0 + 4);
      mcb[k][0] = *(const f32x4*)(mod + 4 * D_ + c0); mcb[k][1] = *(const f32x4*)(mod + 4 * D_ + c0 + 4);
    }
    __builtin_amdgcn_sched_barrier(0);
#pragma unroll
    for (int k = 0; k < 2; ++k) {
      f32x4 s0 = ((pp[k][0][0] + pp[k][1][0]) + (pp[k][2][0] + pp[k][3][0])) + ((pp[k][4][0] + pp[k][5][0]) + (pp[k][6][0] + pp[k][7][0]));
      f32x4 s1 = ((pp[k][0][1] + pp[k][1][1]) + (pp[k][2][1] + pp[k][3][1])) + ((pp[k][4][1] + pp[k][5][1]) + (pp[k][6][1] + pp[k][7][1]));
      f32x4 o0, o1;
#pragma unroll
      for (int e = 0; e < 4; ++e) {
        float x0 = (e & 1) ? bfhi(xx[k][e >> 1]) : bflo(xx[k][e >> 1]);
        float x1v = (e & 1) ? bfhi(xx[k][2 + (e >> 1)]) : bflo(xx[k][2 + (e >> 1)]);
        o0[e] = rbf(x0 + rbf(gg[k][0][e] * rbf(s0[e])));
        o1[e] = rbf(x1v + rbf(gg[k][1][e] * rbf(s1[e])));
      }
      if (k == 0 || two) { const int c0 = k ? c1 : lane * 8; *(f32x4*)(xl + c0) = o0; *(f32x4*)(xl + c0 + 4) = o1; }
    }
  }
#pragma unroll
  for (int rr = 0; rr < 1; ++rr) {
    const int t = t0 + rr;
    float v[16];
    {
      f32x4 p0 = *(const f32x4*)(xl + rr * D_ + lane * 8), p1 = *(const f32x4*)(xl + rr * D_ + lane * 8 + 4);
      f32x4 p2 = *(const f32x4*)(xl + rr * D_ + c1), p3 = *(const f32x4*)(xl + rr * D_ + c1 + 4);
#pragma unroll
      for (int e = 0; e < 4; ++e) { v[e] = p0[e]; v[4 + e] = p1[e]; v[8 + e] = two ? p2[e] : 0.f; v[12 + e] = two ? p3[e] : 0.f; }
    }
    float sm = 0.f;
#pragma unroll
    for (int e = 0; e < 16; ++e) sm += v[e];
    sm = wave_sum(sm);
    const float mean = sm * (1.f / D_);
    float q = 0.f;
#pragma unroll
    for (int e = 0; e < 8; ++e) { float d = v[e] - mean; q += d * d; }
    if (two) {
#pragma unroll
      for (int e = 8; e < 16; ++e) { float d = v[e] - mean; q += d * d; }
    }
    q = wave_sum(q);
    const float mb = rbf(mean), vb = rbf(q * (1.f / D_));
    const float den = rbf(sqrtf(rbf(vb + 1e-5f)));
#pragma unroll
    for (int part = 0; part < 2; ++part) {
      if (part == 0 || two) {
        const int c0 = part * 512 + lane * 8;
        float o[8];
#pragma unroll
        for (int e = 0; e < 8; ++e) {
          float tv = rbf(rbf(v[part * 8 + e] - mb) / den);
          tv = rbf(tv * rbf(1.f + mcb[part][e >> 2][e & 3]));
          o[e] = tv + msb[part][e >> 2][e & 3];
        }
        const int off = ((s * 32 + t) * D_ + c0) * 2;
        v4u hv = {pack2(o[0], o[1]), pack2(o[2], o[3]), pack2(o[4], o[5]), pack2(o[6], o[7])};
        v4u xv = {pack2(v[part * 8 + 0], v[part * 8 + 1]), pack2(v[part * 8 + 2], v[part * 8 + 3]), pack2(v[part * 8 + 4], v[part * 8 + 5]), pack2(v[part * 8 + 6], v[part * 8 + 7])};
        __builtin_amdgcn_raw_buffer_store_b128(hv, rh2, off, 0, 16);
        __builtin_amdgcn_raw_buffer_store_b128(xv, rx1, off, 0, 16);
      }
    }
  }
}

__device__ __forceinline__ void task_f1(const MegaArgs& a, char* smem, int s, int nb) {
  constexpr int BM = 32, BN = 128;
  PGen P; P.A = (const u16*)(a.ws + WS_H2); P.lda = D_; P.W = a.w1; P.W2 = a.w3; P.K = D_; P.ldb = F_;
  const int m0 = s * BM, f0 = nb * 64;
  f32x4 acc[BM / 32][BN / 32];
  gemm_tile_dma<BM, BN, 10, 6, true>(P, m0, f0, smem, acc);
  const int tid = otid(); const int lane = tid & 63, wid = tid >> 6, wm = wid >> 1, wn = wid & 1, fr = lane & 15, g = lane >> 4;
  f32x4* ex = (f32x4*)smem;
  if (wn == 1) {
#pragma unroll
    for (int j = 0; j < BN / 32; ++j) ex[(wm * (BN / 32) + j) * 64 + lane] = acc[0][j];
  }
  __syncthreads();
  if (wn == 0) {
    const __amdgpu_buffer_rsrc_t rg = __builtin_amdgcn_make_buffer_rsrc(a.ws + WS_G, 0, S_ * F_ * 2, 0x00020000);
#pragma unroll
    for (int j = 0; j < BN / 32; j += 2) {
      v2u pk[2];
#pragma unroll
      for (int jj = 0; jj < 2; ++jj) {
        f32x4 up = ex[(wm * (BN / 32) + j + jj) * 64 + lane];
        f32x4 o;
#pragma unroll
        for (int r = 0; r < 4; ++r) {
          float gt = rbf(acc[0][j + jj][r]);
          float sg = rbf(gt / (1.f + __expf(-gt)));
          o[r] = sg * rbf(up[r]);
        }
        pk[jj] = pk4(o);
      }
      v4u v = pair16(pk[0], pk[1]);
      int tok = m0 + wm * (BM / 2) + fr, f = f0 + (j + (g & 1)) * 16 + (g >> 1) * 8;
      __builtin_amdgcn_raw_buffer_store_b128(v, rg, (tok * F_ + f) * 2, 0, 16);
    }
  }
  __syncthreads();
}

__device__ __forceinline__ void task_f2(const MegaArgs& a, char* smem, int s, int nb) {
  constexpr int BM = 32, BN = 64;
  PGen P; P.A = (const u16*)(a.ws + WS_G); P.lda = F_; P.W = a.w2; P.W2 = nullptr; P.K = F_; P.ldb = D_;
  const int m0 = s * BM, n0 = nb * BN;
  f32x4 acc[BM / 32][BN / 32];
  gemm_tile_dma<BM, BN, 24, 8, true>(P, m0, n0, smem, acc);
  const int tid = otid(); const int lane = tid & 63, wid = tid >> 6, wm = wid >> 1, wn = wid & 1, fr = lane & 15, g = lane >> 4;
  const u16* x1 = (const u16*)(a.ws + WS_X1);
  const float* gb = (const float*)(a.ws + WS_MOD) + 5 * D_;
#pragma unroll
  for (int j = 0; j < BN / 32; ++j) {
    int tok = m0 + wm * (BM / 2) + fr, c = n0 + wn * (BN / 2) + j * 16 + 4 * g;
    v2u xv = *(const v2u*)(x1 + (size_t)tok * D_ + c);
    float xo[4] = {bflo(xv[0]), bfhi(xv[0]), bflo(xv[1]), bfhi(xv[1])};
    f32x4 o;
#pragma unroll
    for (int r = 0; r < 4; ++r) o[r] = xo[r] + rbf(gb[c + r] * rbf(acc[0][j][r]));
    *(v2u*)(a.out + (size_t)tok * D_ + c) = pk4(o);
  }
}

constexpr int NP_S = 8 + 24 + 10;
constexpr int D1_ = 8, D2_ = 10;
constexpr int NT2_ = (NSEG_ + D2_) * NP_S;
__device__ __forceinline__ void worker_role(const MegaArgs& a, char* smem, const int x, const int j) {
  unsigned* tmo = ctl(a.ws, C_TMO);
  {
    const int nh = (x == 3) ? 4 : 7, h0 = (x == 3) ? 0 : 4 + (x - 4) * 7, nw = (x == 3) ? 16 : 32;
    const int bq0 = 112 + ((x == 3) ? 0 : 4 + (x - 4) * 11), nbx = (x == 3) ? 4 : 11;
    const int nsplit = 6 * nh + nbx;
    for (int e = j; e < nsplit + (NMT_ - 2) * nh; e += nw) {
      if (e >= 2 * nh && e < 2 * nh + nbx) {
        task_b(a, smem, bq0 + e - 2 * nh);
      } else if (e < nsplit) {
        const int e2 = e < 2 * nh ? e : e - nbx, row = e2 / nh, h = h0 + e2 % nh;
        const int mt = (row == 2 || row == 3 || row == 5) ? 1 : 0;
        if (row == 0 || row == 2) { task_qkvu<6, 3>(a, smem, mt, h); publish(ctl(a.ws, C_K + mt)); }
        else if (row == 1 || row == 3) { task_qkvu<3, 3>(a, smem, mt, h); publish(ctl(a.ws, C_K + mt)); }
        else { task_qkvu<0, 3>(a, smem, mt, h); publish(ctl(a.ws, C_Q0 + mt)); }
      } else {
        const int mt = 2 + (e - nsplit) / nh, h = h0 + (e - nsplit) % nh;
        TSTAMP(mt * 32 + h, 0); TSTAMP(mt * 32 + h, 1);
        task_qkvu<0, 9>(a, smem, mt, h);
        publish(ctl(a.ws, C_K + mt));
        TSTAMP(mt * 32 + h, 2);
      }
    }
  }
  volatile unsigned* lw = (volatile unsigned*)smem;
  for (;;) {
    __syncthreads();
    if (threadIdx.x == 0) lw[0] = __hip_atomic_fetch_add(ctl(a.ws, C_QUEUE), 1u, __ATOMIC_RELAXED, __HIP_MEMORY_SCOPE_AGENT);
    __syncthreads();
    const int t = __builtin_amdgcn_readfirstlane((int)lw[0]);
    __syncthreads();
    if (t >= NT2_) break;
    const int r = t % NP_S;
    const int s = t / NP_S - (r < 8 ? 0 : (r < 32 ? D1_ : D2_));
    if (s < 0 || s >= NSEG_) continue;
    TSTAMP(512 + s * NP_S + r, 0);
    if (r < 8) {
      const int hq = r;
      if (threadIdx.x == 0) { if (s < 8) spin_ge(ctl(a.ws, C_Q0 + (s >> 2)), 32u, tmo); spin_ge(ctl(a.ws, C_K + (s >> 2)), s < 8 ? 64u : 32u, tmo); spin_ge(ctl(a.ws, C_S + s), (unsigned)NCHAIN_, tmo); acquire_all(); }
      __syncthreads();
      TSTAMP(512 + s * NP_S + r, 1);
      task_x(a, smem, s, hq);
      publish(ctl(a.ws, C_T + s));
      if (threadIdx.x == 0) { spin_ge(ctl(a.ws, C_T + s), 8u, tmo); acquire_all(); }
      __syncthreads();
      TSTAMP(512 + s * NP_S + r, 3);
      task_xc(a, smem, s, hq);
      publish(ctl(a.ws, C_XS + s));
    } else if (r < 32) {
      if (threadIdx.x == 0) { spin_ge(ctl(a.ws, C_XS + s), 8u, tmo); acquire_all(); }
      __syncthreads();
      TSTAMP(512 + s * NP_S + r, 1);
      task_f1(a, smem, s, r - 8);
      publish(ctl(a.ws, C_FS + s));
    } else {
      if (threadIdx.x == 0) { spin_ge(ctl(a.ws, C_FS + s), 24u, tmo); acquire_all(); }
      __syncthreads();
      TSTAMP(512 + s * NP_S + r, 1);
      task_f2(a, smem, s, r - 32);
      __syncthreads();
    }
    TSTAMP(512 + s * NP_S + r, 2);
  }
}

__global__ void __launch_bounds__(256, 1) k_mega(MegaArgs a) {
  extern __shared__ __attribute__((aligned(16))) char smem[];
  const int x = blockIdx.x & 7, j = blockIdx.x >> 3;
  if (x < 3 || (x == 3 && j < 16)) {
    const int cidx = (x < 3) ? x * 32 + j : 96 + j;
    if (cidx < H_) score_role(a, smem, cidx);
    else u_role(a, smem, cidx);
  } else {
    worker_role(a, smem, x, x == 3 ? j - 16 : j);
  }
}

extern "C" void kernel_launch(void* const* d_in, const int* in_sizes, int n_in,
                              void* d_out, int out_size, void* d_ws, size_t ws_size,
                              hipStream_t stream) {
  const u16* x = (const u16*)d_in[0];
  const u16* mem0 = (const u16*)d_in[1];
  const u16* Wq = (const u16*)d_in[2];
  const u16* Wk = (const u16*)d_in[3];
  const u16* Wv = (const u16*)d_in[4];
  const u16* Wo = (const u16*)d_in[5];
  const u16* w1 = (const u16*)d_in[8];
  const u16* w3 = (const u16*)d_in[9];
  const u16* w2 = (const u16*)d_in[10];
  const u16* c = (const u16*)d_in[11];
  const u16* wmod = (const u16*)d_in[12];
  char* ws = (char*)d_ws;
  float* mod = (float*)(ws + WS_MOD);

  (void)hipFuncSetAttribute((const void*)k_mega, hipFuncAttributeMaxDynamicSharedMemorySize, MEGA_LDS);

  k_pre<<<240, 256, 0, stream>>>(c, wmod, mem0, ws);
  k_ln<<<S_ / 4, 256, 0, stream>>>(x, mod + 0 * D_, mod + 1 * D_, (u16*)(ws + WS_H1));
  MegaArgs ma; ma.x = x; ma.Wq = Wq; ma.Wk = Wk; ma.Wv = Wv; ma.Wo = Wo; ma.w1 = w1; ma.w3 = w3; ma.w2 = w2; ma.ws = ws; ma.out = (u16*)d_out;
  k_mega<<<NBLK_, 256, MEGA_LDS, stream>>>(ma);
}
```

```cpp
#include <hip/hip_runtime.h>
#include <hip/hip_bf16.h>
#include <stdint.h>

constexpr int S_ = 2048, D_ = 640, H_ = 32, DH_ = 192, NSEG_ = 64, F_ = 1536;
constexpr int HD_ = H_ * DH_;
constexpr int NU_ = 80;
constexpr int UROWS_ = S_ + D_;
constexpr int UCH_ = UROWS_ / 8;
constexpr int NCHAIN_ = H_ + NU_;
constexpr int NBLK_ = 256;
constexpr int NWRK_ = NBLK_ - NCHAIN_;
constexpr int NMT_ = 16;

typedef short bf16x8 __attribute__((ext_vector_type(8)));
typedef short bf16x4 __attribute__((ext_vector_type(4)));
typedef float f32x4 __attribute__((ext_vector_type(4)));
typedef unsigned v4u __attribute__((ext_vector_type(4)));
typedef unsigned v2u __attribute__((ext_vector_type(2)));
typedef unsigned short u16;
typedef short v4i16_t __attribute__((ext_vector_type(4)));

__device__ __forceinline__ float bf2f(u16 x) { return __uint_as_float(((unsigned)x) << 16); }
__device__ __forceinline__ float bflo(unsigned x) { return __uint_as_float(x << 16); }
__device__ __forceinline__ float bfhi(unsigned x) { return __uint_as_float(x & 0xFFFF0000u); }
__device__ __forceinline__ u16 f2bf(float f) { return __builtin_bit_cast(u16, (__bf16)f); }
__device__ __forceinline__ float rbf(float f) { return bf2f(f2bf(f)); }
typedef __bf16 bf2_t __attribute__((ext_vector_type(2)));
typedef float f2_t __attribute__((ext_vector_type(2)));
__device__ __forceinline__ unsigned pack2(float a, float b) { f2_t f = {a, b}; bf2_t r = __builtin_convertvector(f, bf2_t); return __builtin_bit_cast(unsigned, r); }
__device__ __forceinline__ unsigned canon2(unsigned x) { return x == 0xFFFFFFFFu ? 0xFFFFFFFEu : x; }
__device__ __forceinline__ unsigned umax4(unsigned m, v4u v) {
  m = max(max(m, v[0]), v[1]);
  return max(max(m, v[2]), v[3]);
}

constexpr size_t al256(size_t x) { return (x + 255) & ~(size_t)255; }
constexpr size_t WS_CTL  = 0;
constexpr size_t CTL_BYTES = 32768;
constexpr size_t WS_MEMB = CTL_BYTES;
constexpr size_t WS_PB   = WS_MEMB + (size_t)65 * 10240;
constexpr size_t WS_SENT_END = WS_PB + (size_t)64 * 20480;
constexpr size_t WS_MOD  = al256(WS_SENT_END);
constexpr size_t WS_H1   = al256(WS_MOD + 3840 * 4);
constexpr size_t WS_Q    = al256(WS_H1 + (size_t)S_ * D_ * 2);
constexpr size_t WS_K    = WS_Q + (size_t)S_ * HD_ * 2;
constexpr size_t WS_U    = WS_K + (size_t)S_ * HD_ * 2;
constexpr size_t WS_WT   = WS_U + (size_t)H_ * UCH_ * D_ * 8 * 2;
constexpr size_t WS_KM   = WS_WT + (size_t)2 * D_ * HD_ * 2;
constexpr size_t WS_UM   = WS_KM + (size_t)NSEG_ * 8 * HD_ * 2;
constexpr size_t WS_X1   = WS_UM + (size_t)NSEG_ * H_ * D_ * 8 * 2;
constexpr size_t WS_H2   = WS_X1 + (size_t)S_ * D_ * 2;
constexpr size_t WS_G    = WS_H2 + (size_t)S_ * D_ * 2;
constexpr size_t WS_XP   = WS_G + (size_t)S_ * F_ * 2;
constexpr size_t WS_END  = WS_XP + (size_t)NSEG_ * 8 * 32 * D_ * 4;
constexpr int C_TMO = 0, C_K = 1, C_X = 49, C_F = 65, C_S = 81, C_T = 145, C_QUEUE = 210, C_XS = 256, C_FS = 320, C_Q0 = 211, C_B = 213;
__device__ __forceinline__ unsigned* ctl(char* ws, int idx) { return (unsigned*)(ws + WS_CTL) + idx * 16; }

constexpr unsigned SPIN_CAP = 1u << 22;
#define SPIN_SLEEP 12

__device__ __forceinline__ void spin_ge(unsigned* c, unsigned target, unsigned* tmo) {
  unsigned spins = 0;
  while (__hip_atomic_load(c, __ATOMIC_RELAXED, __HIP_MEMORY_SCOPE_AGENT) < target) {
    __builtin_amdgcn_s_sleep(SPIN_SLEEP);
    if (++spins > SPIN_CAP) { __hip_atomic_store(tmo, 1u, __ATOMIC_RELAXED, __HIP_MEMORY_SCOPE_AGENT); break; }
  }
}
__device__ __forceinline__ void acquire_all() {
  __builtin_amdgcn_fence(__ATOMIC_ACQUIRE, "agent");
  asm volatile("s_waitcnt vmcnt(0)" ::: "memory");
}
__device__ __forceinline__ void publish(unsigned* c) {
  asm volatile("s_waitcnt vmcnt(0)" ::: "memory");
  __syncthreads();
  if (threadIdx.x == 64) __hip_atomic_fetch_add(c, 1u, __ATOMIC_RELAXED, __HIP_MEMORY_SCOPE_AGENT);
  __syncthreads();
}

__device__ __forceinline__ v4u pair16(v2u A, v2u B) {
  auto r0 = __builtin_amdgcn_permlane16_swap(A[0], B[0], false, false);
  auto r1 = __builtin_amdgcn_permlane16_swap(A[1], B[1], false, false);
  unsigned a0 = r0[0], b0 = r0[1], a1 = r1[0], b1 = r1[1];
  return (v4u){a0, a1, b0, b1};
}
__device__ __forceinline__ int otid() { int t = threadIdx.x; asm volatile("" : "+v"(t)); return t; }
__device__ __forceinline__ v2u pk4(f32x4 v) { return (v2u){pack2(v[0], v[1]), pack2(v[2], v[3])}; }

__global__ void __launch_bounds__(256) k_ln(const u16* __restrict__ x, const float* __restrict__ shift, const float* __restrict__ scale, u16* __restrict__ y) {
  const int lane = threadIdx.x & 63, w = threadIdx.x >> 6;
  const int row = blockIdx.x * 4 + w;
  const unsigned* xr = (const unsigned*)(x + (size_t)row * D_);
  float v[10];
  float s = 0.f;
#pragma unroll
  for (int i = 0; i < 5; ++i) { unsigned u = xr[lane + 64 * i]; v[2 * i] = bflo(u); v[2 * i + 1] = bfhi(u); s += v[2 * i] + v[2 * i + 1]; }
#pragma unroll
  for (int m = 32; m >= 1; m >>= 1) s += __shfl_xor(s, m);
  const float mean = s * (1.f / D_);
  float q = 0.f;
#pragma unroll
  for (int i = 0; i < 10; ++i) { float d = v[i] - mean; q += d * d; }
#pragma unroll
  for (int m = 32; m >= 1; m >>= 1) q += __shfl_xor(q, m);
  const float mb = rbf(mean), vb = rbf(q * (1.f / D_));
  const float den = rbf(sqrtf(rbf(vb + 1e-5f)));
  unsigned* yr = (unsigned*)(y + (size_t)row * D_);
#pragma unroll
  for (int i = 0; i < 5; ++i) {
    int c = 2 * (lane + 64 * i);
    float o[2];
#pragma unroll
    for (int e = 0; e < 2; ++e) {
      float t = rbf(rbf(v[2 * i + e] - mb) / den);
      t = rbf(t * rbf(1.f + scale[c + e]));
      o[e] = t + shift[c + e];
    }
    yr[lane + 64 * i] = pack2(o[0], o[1]);
  }
}

__device__ __forceinline__ int fswz(int r) { return (((r >> 1) & 1) << 1) | (((r >> 3) & 1) << 2); }

__device__ __forceinline__ bf16x8 tr_frag(const char* sub_base, int krow0, int c0, int lane) {
  const int g = lane >> 4, q = (lane & 15) >> 2, pp = lane & 3;
  v4i16_t lo, hi;
  { int r = krow0 + 8 * g + q; lo = __builtin_amdgcn_ds_read_tr16_b64_v4i16((__attribute__((address_space(3))) v4i16_t*)(sub_base + r * 128 + (((c0 + (pp >> 1)) ^ fswz(r)) * 16) + 8 * (pp & 1))); }
  { int r = krow0 + 8 * g + 4 + q; hi = __builtin_amdgcn_ds_read_tr16_b64_v4i16((__attribute__((address_space(3))) v4i16_t*)(sub_base + r * 128 + (((c0 + (pp >> 1)) ^ fswz(r)) * 16) + 8 * (pp & 1))); }
  return (bf16x8){lo[0], lo[1], lo[2], lo[3], hi[0], hi[1], hi[2], hi[3]};
}

__device__ __forceinline__ void glds16(const void* gsrc, unsigned lds_dst) {
  unsigned keep;
  asm volatile("s_mov_b32 %0, m0\n\ts_mov_b32 m0, %2\n\ts_nop 0\n\tglobal_load_lds_dwordx4 %1, off\n\ts_mov_b32 m0, %0"
               : "=&s"(keep) : "v"(gsrc), "s"(lds_dst) : "memory");
}
#define WAITV(n) asm volatile("s_waitcnt vmcnt(%0)" :: "n"(n) : "memory")
#define RAWBAR() do { asm volatile("s_waitcnt lgkmcnt(0)" ::: "memory"); __builtin_amdgcn_s_barrier(); asm volatile("" ::: "memory"); } while (0)
template <int BM, int BN, int NK, int NBUF, bool SWAP, class PT>
__device__ __forceinline__ void gemm_tile_dma(const PT& P, int m0, int n0, char* smem, f32x4 (&acc)[BM / 32][BN / 32]) {
  constexpr int ABYTES = BM * 128, BBYTES = BN * 128, STG = ABYTES + BBYTES;
  constexpr int NA = BM * 8 / 256, NB = BN * 8 / 256, NOP = NA + NB;
  const int tid = otid(), lane = tid & 63, wid = tid >> 6, wm = wid >> 1, wn = wid & 1;
  const u16* asrc[NA];
  const u16* bsrc[NB];
#pragma unroll
  for (int it = 0; it < NA; ++it) { int p = it * 256 + tid; int row = p >> 3, slot = p & 7; asrc[it] = P.a_row(m0 + row) + ((slot ^ (row & 7)) * 8); }
#pragma unroll
  for (int it = 0; it < NB; ++it) { int p = it * 256 + tid; int sub = p >> 9, r = (p >> 3) & 63, slot = p & 7; bsrc[it] = P.b_col(n0, sub) + (size_t)r * P.ldb + ((slot ^ fswz(r)) * 8); }
  const unsigned ldsw = __builtin_amdgcn_readfirstlane((unsigned)(size_t)smem + (tid >> 6) * 1024);
  auto stage_op = [&](int kt, int op) {
    const unsigned base = ldsw + (kt % NBUF) * STG;
    if (op < NA) glds16(asrc[op] + kt * 64, base + op * 4096);
    else glds16(bsrc[op - NA] + (size_t)kt * 64 * P.ldb, base + ABYTES + (op - NA) * 4096);
  };
#pragma unroll
  for (int i = 0; i < BM / 32; ++i)
#pragma unroll
    for (int j = 0; j < BN / 32; ++j) acc[i][j] = (f32x4){0.f, 0.f, 0.f, 0.f};
  const int fr = lane & 15, g = lane >> 4;
#pragma unroll
  for (int d = 0; d < NBUF - 1; ++d)
#pragma unroll
    for (int op = 0; op < NOP; ++op) stage_op(d, op);
  constexpr int NI = BM / 32, NJ = BN / 32, NM = 2 * NI * NJ;
  constexpr int EVERY = NM / NOP;
#pragma unroll
  for (int kt = 0; kt < NK; ++kt) {
    const int after = (kt + NBUF - 2 < NK - 1 ? NBUF - 2 : NK - 1 - kt);
    if (after == 0) WAITV(0); else if (after == 1) WAITV(NOP); else if (after == 2) WAITV(2 * NOP); else if (after == 3) WAITV(3 * NOP); else if (after == 4) WAITV(4 * NOP); else if (after == 5) WAITV(5 * NOP); else WAITV(6 * NOP);
    RAWBAR();
    char* base = smem + (kt % NBUF) * STG;
    char* bbase = base + ABYTES;
    bf16x8 af[2][NI], bf[2][NJ];
#pragma unroll
    for (int kk = 0; kk < 2; ++kk) {
#pragma unroll
      for (int j = 0; j < NJ; ++j) {
        int col = wn * (BN / 2) + j * 16;
        bf[kk][j] = tr_frag(bbase + (col >> 6) * 8192, kk * 32, (col & 63) >> 3, lane);
      }
#pragma unroll
      for (int i = 0; i < NI; ++i) {
        int row = wm * (BM / 2) + i * 16 + fr;
        af[kk][i] = *(const bf16x8*)(base + row * 128 + (((kk * 4 + g) ^ (row & 7)) * 16));
      }
    }
    __builtin_amdgcn_sched_barrier(0);
    if (kt + NBUF - 1 < NK) stage_op(kt + NBUF - 1, 0);
#pragma unroll
    for (int m = 0; m < NM; ++m) {
      const int kk = m / (NI * NJ), i = (m / NJ) % NI, j = m % NJ;
      acc[i][j] = SWAP ? __builtin_amdgcn_mfma_f32_16x16x32_bf16(bf[kk][j], af[kk][i], acc[i][j], 0, 0, 0)
                       : __builtin_amdgcn_mfma_f32_16x16x32_bf16(af[kk][i], bf[kk][j], acc[i][j], 0, 0, 0);
      if ((m + 1) % EVERY == 0 && (m + 1) / EVERY < NOP) {
        __builtin_amdgcn_sched_barrier(0);
        if (kt + NBUF - 1 < NK) stage_op(kt + NBUF - 1, (m + 1) / EVERY);
        __builtin_amdgcn_sched_barrier(0);
      }
    }
    __builtin_amdgcn_sched_barrier(0);
  }
  RAWBAR();
}

struct PGen {
  const u16* A; const u16* W; const u16* W2; int lda; int K; int ldb;
  __device__ const u16* a_row(int m) const { return A + (size_t)m * lda; }
  __device__ const u16* b_col(int n0, int sub) const { return W2 ? (sub ? W2 : W) + n0 : W + n0 + sub * 64; }
};

__global__ void __launch_bounds__(256) k_pre(const u16* __restrict__ c, const u16* __restrict__ wmod, const u16* __restrict__ mem0, char* ws) {
  __shared__ float red[16][17];
  __shared__ float sv[D_];
  const int b = blockIdx.x, tid = threadIdx.x;
  for (int k = tid; k < D_; k += 256) { float cv = bf2f(c[k]); sv[k] = rbf(cv / (1.f + __expf(-cv))); }
  if (b == 0) {
    u16* memb = (u16*)(ws + WS_MEMB);
    for (int gid = tid; gid < 640; gid += 256) { int u = gid >> 3, i = gid & 7; *(v4u*)(memb + gid * 8) = *(const v4u*)(mem0 + i * D_ + u * 8); }
  }
  {
    const v4u z = {0u, 0u, 0u, 0u}, f = {0xFFFFFFFFu, 0xFFFFFFFFu, 0xFFFFFFFFu, 0xFFFFFFFFu};
    for (size_t o = (size_t)(b * 256 + tid) * 16; o < CTL_BYTES; o += (size_t)240 * 256 * 16) *(v4u*)(ws + WS_CTL + o) = z;
    for (size_t o = (size_t)(b * 256 + tid) * 16; o < WS_SENT_END - WS_MEMB - 10240; o += (size_t)240 * 256 * 16) *(v4u*)(ws + WS_MEMB + 10240 + o) = f;
  }
  __syncthreads();
  const int col = tid & 15, kg = tid >> 4, n = b * 16 + col;
  float acc = 0.f;
#pragma unroll
  for (int k = 0; k < 40; ++k) acc += sv[kg * 40 + k] * bf2f(wmod[(size_t)(kg * 40 + k) * 3840 + n]);
  red[kg][col] = acc;
  __syncthreads();
  if (tid < 16) {
    float t = 0.f;
#pragma unroll
    for (int q = 0; q < 16; ++q) t += red[q][tid];
    ((float*)(ws + WS_MOD))[b * 16 + tid] = rbf(t);
  }
}

struct MegaArgs {
  const u16* x; const u16* Wq; const u16* Wk; const u16* Wv; const u16* Wo; const u16* w1; const u16* w3; const u16* w2;
  char* ws; u16* out;
};
constexpr int MEGA_LDS = 160 * 1024;
#define USLEEP 8
#define STAMP(id)
#define TSTAMP(t, id)
#define MFMA_AA(acc, wa, mb) asm volatile("v_mfma_f32_16x16x32_bf16 %0, %1, %2, %0" : "+v"(acc) : "a"(wa), "v"(mb))
#define MFMA_VV(acc, wa, mb) asm volatile("v_mfma_f32_16x16x32_bf16 %0, %1, %2, %0" : "+v"(acc) : "v"(wa), "v"(mb))
#define MFMA_BA(acc, ma, wb) asm volatile("v_mfma_f32_16x16x32_bf16 %0, %1, %2, %0" : "+v"(acc) : "v"(ma), "a"(wb))
#define MFMA_ACCA(acc, ma, wb) asm("v_mfma_f32_16x16x32_bf16 %0, %1, %2, %0" : "+a"(acc) : "v"(ma), "v"(wb))
#define MFMA_ACCV(acc, ma, wb) asm("v_mfma_f32_16x16x32_bf16 %0, %1, %2, %0" : "+v"(acc) : "v"(ma), "v"(wb))
#define MFMA16(acc, ma, wb) asm volatile("v_mfma_f32_16x16x16_bf16 %0, %1, %2, %0" : "+v"(acc) : "v"(ma), "v"(wb))

#define THROTTLE 0
#define PRE_U 3
#define PRE_NUM 3
__device__ __forceinline__ void presleep(unsigned long long t0, unsigned pre) {
  while ((unsigned)(__builtin_amdgcn_s_memtime() - t0) < pre) __builtin_amdgcn_s_sleep(2);
}
template <int NUM = PRE_NUM>
__device__ __forceinline__ unsigned nextpre(unsigned long long t0, unsigned& wmin) {
  unsigned wdur = (unsigned)(__builtin_amdgcn_s_memtime() - t0);
  wmin = min(wmin, wdur);
  return NUM * (wmin >> 3);
}
__device__ __forceinline__ v4u ror8(v4u v) {
  v4u r;
#pragma unroll
  for (int i = 0; i < 4; ++i) r[i] = (unsigned)__builtin_amdgcn_update_dpp(0, (int)v[i], 0x128, 0xf, 0xf, false);
  return r;
}
__device__ __forceinline__ float xmax8(float x) { return fmaxf(x, __builtin_bit_cast(float, __builtin_amdgcn_update_dpp(0, __builtin_bit_cast(int, x), 0x128, 0xf, 0xf, false))); }
__device__ __forceinline__ float xadd8(float x) { return x + __builtin_bit_cast(float, __builtin_amdgcn_update_dpp(0, __builtin_bit_cast(int, x), 0x128, 0xf, 0xf, false)); }
template <int N> __device__ __forceinline__ float dpp_ror_add(float x) { return x + __builtin_bit_cast(float, __builtin_amdgcn_update_dpp(0, __builtin_bit_cast(int, x), 0x120 + N, 0xf, 0xf, false)); }
__device__ __forceinline__ void swap16(float x, float& a, float& b) { auto r = __builtin_amdgcn_permlane16_swap(__float_as_uint(x), __float_as_uint(x), false, false); unsigned r0 = r[0], r1 = r[1]; a = __uint_as_float(r0); b = __uint_as_float(r1); }
__device__ __forceinline__ void swap32(float x, float& a, float& b) { auto r = __builtin_amdgcn_permlane32_swap(__float_as_uint(x), __float_as_uint(x), false, false); unsigned r0 = r[0], r1 = r[1]; a = __uint_as_float(r0); b = __uint_as_float(r1); }
__device__ __forceinline__ float xmax16(float x) { float a, b; swap16(x, a, b); return fmaxf(a, b); }
__device__ __forceinline__ float xadd16(float x) { float a, b; swap16(x, a, b); return a + b; }
__device__ __forceinline__ float xmax32(float x) { float a, b; swap32(x, a, b); return fmaxf(a, b); }
__device__ __forceinline__ float xadd32(float x) { float a, b; swap32(x, a, b); return a + b; }
__device__ __forceinline__ float wave_sum(float x) { x = dpp_ror_add<8>(x); x = dpp_ror_add<4>(x); x = dpp_ror_add<2>(x); x = dpp_ror_add<1>(x); return xadd32(xadd16(x)); }

__device__ __forceinline__ void wo_op_(const MegaArgs& a, char* smem, int tid, int h, int pass, int it) {
  const u16* wosrc = a.Wo + (size_t)h * DH_ * D_ + (size_t)(tid >> 3) * D_ + (((tid & 7) ^ fswz(tid >> 3)) * 8);
  const unsigned ldsw = __builtin_amdgcn_readfirstlane((unsigned)(size_t)smem + (tid >> 6) * 1024);
  glds16(wosrc + (size_t)it * 32 * D_ + pass * 64, ldsw + (pass & 3) * 24576 + it * 4096);
}
template <int NST0, int NST1>
__device__ __forceinline__ void u_gemm(const MegaArgs& a, char* smem, const int tid, const int urow0, const int h) {
  const int lane = tid & 63, wid = tid >> 6, wm = wid >> 1, wn = wid & 1, fr = lane & 15, g = lane >> 4;
  char* vimg = smem + 96 * 1024;
  const __amdgpu_buffer_rsrc_t ru = __builtin_amdgcn_make_buffer_rsrc(a.ws + WS_U, 0, H_ * UCH_ * D_ * 16, 0x00020000);
#pragma unroll
  for (int pass = 0; pass < 10; ++pass) {
    constexpr int N0[10] = {12, 16, 20, 24, 24, 24, 24, 24, 18, 12};
    if (pass < 3 && NST0 != NST1) { if (wn == 0) WAITV(N0[pass] + NST0); else WAITV(N0[pass] + NST1); } else if (pass < 3) WAITV(N0[pass] + NST0); else WAITV(N0[pass]);
    RAWBAR();
    char* bb = smem + (pass & 3) * 24576;
    f32x4 acc[4][2];
#pragma unroll
    for (int i = 0; i < 4; ++i) { acc[i][0] = (f32x4){0.f, 0.f, 0.f, 0.f}; acc[i][1] = (f32x4){0.f, 0.f, 0.f, 0.f}; }
    bf16x8 af[2][4], bf[2][2];
    auto rdfr = [&](int ks, int sl) {
      const int kt = ks >> 1, kk = ks & 1;
#pragma unroll
      for (int j = 0; j < 2; ++j) bf[sl][j] = tr_frag(bb + kt * 8192, kk * 32, (wn * 32 + j * 16) >> 3, lane);
#pragma unroll
      for (int i = 0; i < 4; ++i) { int row = wm * 64 + i * 16 + fr; af[sl][i] = *(const bf16x8*)(vimg + kt * 16384 + row * 128 + (((kk * 4 + g) ^ (row & 7)) * 16)); }
    };
    rdfr(0, 0);
#pragma unroll
    for (int ks = 0; ks < 6; ++ks) {
      if (ks + 1 < 6) rdfr(ks + 1, (ks + 1) & 1);
      if (pass + 3 < 10) wo_op_(a, smem, tid, h, pass + 3, ks);
      __builtin_amdgcn_sched_barrier(0);
#pragma unroll
      for (int i = 0; i < 4; ++i)
#pragma unroll
        for (int j = 0; j < 2; ++j) acc[i][j] = __builtin_amdgcn_mfma_f32_16x16x32_bf16(af[ks & 1][i], bf[ks & 1][j], acc[i][j], 0, 0, 0);
      __builtin_amdgcn_sched_barrier(0);
    }
#pragma unroll
    for (int i = 0; i < 4; i += 2)
#pragma unroll
      for (int j = 0; j < 2; ++j) {
        v4u v = pair16(pk4(acc[i][j]), pk4(acc[i + 1][j]));
        int row = urow0 + wm * 64 + (i + (g & 1)) * 16 + (g >> 1) * 8, c = pass * 64 + wn * 32 + j * 16 + fr;
        __builtin_amdgcn_raw_buffer_store_b128(v, ru, ((h * UCH_ + (row >> 3)) * D_ + c) * 16, 0, 16);
      }
  }
}

__device__ __forceinline__ void task_b(const MegaArgs& a, char* smem, const int q) {
  const int mb = q % 5, hh = q / 5;
  const int t_ = otid();
  const unsigned ldsw = __builtin_amdgcn_readfirstlane((unsigned)(size_t)smem + (t_ >> 6) * 1024);
#pragma unroll
  for (int it = 0; it < 12; ++it) {
    const int row = (it & 3) * 32 + (t_ >> 3);
    glds16(a.Wv + (size_t)(mb * 128 + row) * HD_ + hh * DH_ + (it >> 2) * 64 + (((t_ & 7) ^ (row & 7)) * 8), ldsw + 96 * 1024 + it * 4096);
  }
#pragma unroll
  for (int ps = 0; ps < 3; ++ps)
#pragma unroll
    for (int it = 0; it < 6; ++it) wo_op_(a, smem, t_, hh, ps, it);
  u_gemm<0, 0>(a, smem, t_, S_ + mb * 128, hh);
  publish(ctl(a.ws, C_B));
}

__device__ __forceinline__ void score_role(const MegaArgs& a, char* smem, const int b) {
  const int tid = threadIdx.x, lane = tid & 63, w = __builtin_amdgcn_readfirstlane(tid >> 6);
  const int fr = lane & 15, g = lane >> 4;
  const __amdgpu_buffer_rsrc_t rm = __builtin_amdgcn_make_buffer_rsrc(a.ws + WS_MEMB, 0, 65 * 10240, 0x00020000);
  const __amdgpu_buffer_rsrc_t rp = __builtin_amdgcn_make_buffer_rsrc(a.ws + WS_PB, 0, 64 * 20480, 0x00020000);
  const __amdgpu_buffer_rsrc_t rkm = __builtin_amdgcn_make_buffer_rsrc(a.ws + WS_KM, 0, NSEG_ * 8 * HD_ * 2, 0x00020000);
  const u16* Kx = (const u16*)(a.ws + WS_K);
  unsigned* tmo = ctl(a.ws, C_TMO);
  const int moff2 = (fr >> 3) * 512 + (g * 8 + (fr & 7)) * 16;
  const int h = b;
  char* lw = smem;
  float* part = (float*)(smem + 144 * 1024);
  char* lmem = smem + 149 * 1024;
  task_b(a, smem, NU_ + h);
  bf16x8 wa[64], wv[20];
  {
    const unsigned ldsw = __builtin_amdgcn_readfirstlane((unsigned)(size_t)smem + w * 1024);
    const size_t boff = (size_t)(tid >> 3) * HD_ + h * DH_ + (((tid & 7) ^ fswz(tid >> 3)) * 8);
    auto stage = [&](int ks) {
#pragma unroll
      for (int it = 0; it < 6; ++it) glds16((it < 3 ? a.Wq : a.Wk) + boff + (size_t)(ks * 32) * HD_ + (it % 3) * 64, ldsw + (ks & 3) * 24576 + it * 4096);
    };
    bf16x8* scr = (bf16x8*)(a.ws + WS_WT) + ((size_t)(h * 4 + w) * 36) * 64 + lane;
    stage(0); stage(1); stage(2);
#pragma unroll
    for (int ks = 0; ks < 20; ++ks) {
      if (ks < 18) WAITV(12); else if (ks == 18) WAITV(6); else WAITV(0);
      RAWBAR();
      if (ks + 3 < 20) {
        stage(ks + 3);
      }
#pragma unroll
      for (int p = 0; p < 6; ++p) {
        const int etg = 3 * w + (p % 3);
        bf16x8 v = tr_frag(smem + (ks & 3) * 24576 + (p / 3) * 12288 + (etg >> 2) * 4096, 0, (etg & 3) * 2, lane);
        const int f = p * 20 + ks;
        if (f < 64) wa[f] = v; else if (f < 84) wv[f - 64] = v; else scr[(f - 84) * 64] = v;
      }
    }
    asm volatile("s_waitcnt vmcnt(0)" ::: "memory");
    __syncthreads();
    for (int f = 0; f < 36; ++f) *(bf16x8*)(lw + (w * 36 + f) * 1024 + lane * 16) = scr[f * 64];
  }
  const char* lwp = lw + w * 36 * 1024 + lane * 16;
  const float scale = 0.07216878364870322f;
  unsigned pre_m = 0, wmin_m = 0xFFFFFFFFu;
  for (int s = 0; s < NSEG_; ++s) {
    if ((s & 3) == 0) {
      if (tid == 0) { spin_ge(ctl(a.ws, C_K + (s >> 2)), s < 8 ? 64u : 32u, tmo); acquire_all(); }
      __syncthreads();
    }
    STAMP(0);
    bf16x4 kx[2][3];
#pragma unroll
    for (int kt = 0; kt < 2; ++kt)
#pragma unroll
      for (int et = 0; et < 3; ++et)
        kx[kt][et] = *(const bf16x4*)(Kx + (size_t)(s * 32 + kt * 16 + fr) * HD_ + h * DH_ + (3 * w + et) * 16 + 4 * g);
    bf16x8 l5[2];
    l5[0] = *(const bf16x8*)(lwp + 16 * 1024);
    l5[1] = *(const bf16x8*)(lwp + 17 * 1024);
    {
      v4u mv[3];
      unsigned spins = 0;
      const unsigned long long t0 = __builtin_amdgcn_s_memtime();
      presleep(t0, pre_m);
      for (;;) {
        unsigned mx = 0;
#pragma unroll
        for (int k = 0; k < 3; ++k) { const int c = tid + 256 * k; mv[k] = __builtin_amdgcn_raw_buffer_load_b128(rm, s * 10240 + (c < 640 ? c : tid) * 16, 0, 16); }
#pragma unroll
        for (int k = 0; k < 3; ++k) mx = umax4(mx, mv[k]);
        if (!__any(mx == 0xFFFFFFFFu)) break;
        if (++spins > SPIN_CAP) { __hip_atomic_store(tmo, 1u, __ATOMIC_RELAXED, __HIP_MEMORY_SCOPE_AGENT); break; }
      }
      pre_m = nextpre(t0, wmin_m);
#pragma unroll
      for (int k = 0; k < 3; ++k) { const int c = tid + 256 * k; if (c < 640) *(v4u*)(lmem + c * 16) = mv[k]; }
    }
    __syncthreads();
    STAMP(1);
    f32x4 acc[6];
#pragma unroll
    for (int p = 0; p < 6; ++p) acc[p] = (f32x4){0.f, 0.f, 0.f, 0.f};
    bf16x8 l4[2], mf[3];
    l4[0] = *(const bf16x8*)(lwp + 0 * 1024);
    l4[1] = *(const bf16x8*)(lwp + 1 * 1024);
    const char* lmp = lmem + (g * 8 + (fr & 7)) * 16;
    mf[0] = *(const bf16x8*)(lmp); mf[1] = *(const bf16x8*)(lmp + 512); mf[2] = *(const bf16x8*)(lmp + 1024);
    asm volatile("s_nop 1");
#pragma unroll
    for (int ks = 0; ks < 20; ++ks) {
      const bf16x8 m = mf[ks % 3];
      MFMA_AA(acc[0], wa[ks], m);
      MFMA_AA(acc[1], wa[20 + ks], m);
      MFMA_AA(acc[2], wa[40 + ks], m);
      if (ks < 4) MFMA_AA(acc[3], wa[60 + ks], m); else MFMA_VV(acc[3], wv[ks - 4], m);
      if (ks < 4) MFMA_VV(acc[4], wv[16 + ks], m);
      else { MFMA_VV(acc[4], l4[ks & 1], m); if (ks + 2 < 20) l4[ks & 1] = *(const bf16x8*)(lwp + (ks + 2 - 4) * 1024); }
      MFMA_VV(acc[5], l5[ks & 1], m);
      if (ks + 2 < 20) l5[ks & 1] = *(const bf16x8*)(lwp + (16 + ks + 2) * 1024);
      if (ks + 3 < 20) mf[ks % 3] = *(const bf16x8*)(lmp + (ks + 3) * 512);
    }
    asm volatile("s_nop 7\n\ts_nop 1" : "+v"(acc[0]), "+v"(acc[1]), "+v"(acc[2]), "+v"(acc[3]), "+v"(acc[4]), "+v"(acc[5]));
    STAMP(3);
    bf16x4 qf[3], kf[3];
#pragma unroll
    for (int et = 0; et < 3; ++et) { qf[et] = __builtin_bit_cast(bf16x4, pk4(acc[et])); kf[et] = __builtin_bit_cast(bf16x4, pk4(acc[3 + et])); }
    f32x4 st[3];
#pragma unroll
    for (int kt = 0; kt < 3; ++kt) st[kt] = (f32x4){0.f, 0.f, 0.f, 0.f};
    asm volatile("s_nop 1" : "+v"(qf[0]), "+v"(qf[1]), "+v"(qf[2]), "+v"(kf[0]), "+v"(kf[1]), "+v"(kf[2]), "+v"(st[0]), "+v"(st[1]), "+v"(st[2]));
#pragma unroll
    for (int et = 0; et < 3; ++et) {
      MFMA16(st[0], kf[et], qf[et]);
      MFMA16(st[1], kx[0][et], qf[et]);
      MFMA16(st[2], kx[1][et], qf[et]);
    }
    asm volatile("s_nop 7\n\ts_nop 1" : "+v"(st[0]), "+v"(st[1]), "+v"(st[2]));
    float* pt = part;
    if (fr < 8) {
      if (g < 2) *(f32x4*)(pt + (w * 8 + fr) * 40 + 4 * g) = st[0];
      *(f32x4*)(pt + (w * 8 + fr) * 40 + 8 + 4 * g) = st[1];
      *(f32x4*)(pt + (w * 8 + fr) * 40 + 24 + 4 * g) = st[2];
    }
    STAMP(4);
    __syncthreads();
    STAMP(5);
    if (w == 0) {
      const int i = lane & 7, jc = lane >> 3;
      const int jr = jc < 5 ? jc : 0;
      float sc[8];
      f32x4 pp[8];
#pragma unroll
      for (int ww = 0; ww < 4; ++ww) { pp[2 * ww] = *(const f32x4*)(pt + (ww * 8 + i) * 40 + jr * 8); pp[2 * ww + 1] = *(const f32x4*)(pt + (ww * 8 + i) * 40 + jr * 8 + 4); }
      __builtin_amdgcn_sched_barrier(0);
#pragma unroll
      for (int e = 0; e < 4; ++e) { sc[e] = (pp[0][e] + pp[2][e]) + (pp[4][e] + pp[6][e]); sc[4 + e] = (pp[1][e] + pp[3][e]) + (pp[5][e] + pp[7][e]); }
      float mx = -3.0e38f;
#pragma unroll
      for (int e = 0; e < 8; ++e) { sc[e] = rbf(rbf(sc[e]) * scale); mx = fmaxf(mx, sc[e]); }
      mx = xmax8(mx); mx = xmax16(mx); mx = xmax32(mx);
      float sum = 0.f;
#pragma unroll
      for (int e = 0; e < 8; ++e) { sc[e] = __expf(sc[e] - mx); sum += sc[e]; }
      if (jc >= 5) sum = 0.f;
      sum = xadd8(sum); sum = xadd16(sum); sum = xadd32(sum);
      if (jc < 5) {
        const float inv = 1.f / sum;
        v4u o = {canon2(pack2(sc[0] * inv, sc[1] * inv)), canon2(pack2(sc[2] * inv, sc[3] * inv)), canon2(pack2(sc[4] * inv, sc[5] * inv)), canon2(pack2(sc[6] * inv, sc[7] * inv))};
        const int kc = (jc == 0) ? 128 + h : h * 4 + jc - 1;
        __builtin_amdgcn_raw_buffer_store_b128(o, rp, s * 20480 + kc * 128 + i * 16, 0, 16);
      }
    } else if (tid == 64 && s > 0) {
      __hip_atomic_fetch_add(ctl(a.ws, C_S + s - 1), 1u, __ATOMIC_RELAXED, __HIP_MEMORY_SCOPE_AGENT);
    }
    if (fr < 8) {
#pragma unroll
      for (int et = 0; et < 3; ++et)
        __builtin_amdgcn_raw_buffer_store_b64(__builtin_bit_cast(v2u, kf[et]), rkm, ((s * 8 + fr) * HD_ + h * DH_ + (3 * w + et) * 16 + 4 * g) * 2, 0, 16);
    }
    STAMP(6);
    __syncthreads();
  }
  publish(ctl(a.ws, C_S + NSEG_ - 1));
}

__device__ __forceinline__ void u_role(const MegaArgs& a, char* smem, const int b) {
  const int tid = threadIdx.x, lane = tid & 63, w = __builtin_amdgcn_readfirstlane(tid >> 6);
  const int fr = lane & 15, g = lane >> 4;
  const __amdgpu_buffer_rsrc_t rm = __builtin_amdgcn_make_buffer_rsrc(a.ws + WS_MEMB, 0, 65 * 10240, 0x00020000);
  const __amdgpu_buffer_rsrc_t rp = __builtin_amdgcn_make_buffer_rsrc(a.ws + WS_PB, 0, 64 * 20480, 0x00020000);
  const __amdgpu_buffer_rsrc_t rum = __builtin_amdgcn_make_buffer_rsrc(a.ws + WS_UM, 0, NSEG_ * H_ * D_ * 16, 0x00020000);
  const u16* U = (const u16*)(a.ws + WS_U);
  unsigned* tmo = ctl(a.ws, C_TMO);
  const int moff2 = (fr >> 3) * 512 + (g * 8 + (fr & 7)) * 16;
  const int u = b - H_;
  char* lub = smem;
  float* red = (float*)(smem + 4096);
  task_b(a, smem, u);
  if (tid == 0) { spin_ge(ctl(a.ws, C_B), 160u, tmo); acquire_all(); }
  __syncthreads();
  bf16x8 ba[64], bv[16];
#pragma unroll
  for (int f = 0; f < 80; ++f) {
    const int nt = f / 20, ks = f % 20;
    const int hh = (4 * w + nt) * 2 + (fr >> 3), c = 8 * u + (fr & 7);
    bf16x8 v = *(const bf16x8*)(U + (((size_t)hh * UCH_ + 256 + ks * 4 + g) * D_ + c) * 8);
    if (f < 64) ba[f] = v; else bv[f - 64] = v;
  }
  unsigned pre_m = 0, pre_p = 0, wmin_m = 0xFFFFFFFFu, wmin_p = 0xFFFFFFFFu;
  for (int s = 0; s < NSEG_; ++s) {
    if ((s & 3) == 0) {
      if (tid == 0) { spin_ge(ctl(a.ws, C_K + (s >> 2)), s < 8 ? 64u : 32u, tmo); acquire_all(); }
      __syncthreads();
    }
    STAMP(0);
    bf16x8 ub[10];
#pragma unroll
    for (int k = 0; k < 8; ++k) ub[k] = *(const bf16x8*)(U + (((size_t)(8 * w + k) * UCH_ + s * 4 + g) * D_ + 8 * u + (fr & 7)) * 8);
    v4u mv[20];
    {
      unsigned spins = 0;
      const unsigned long long t0 = __builtin_amdgcn_s_memtime();
      presleep(t0, pre_m);
      for (;;) {
        __builtin_amdgcn_s_sleep(USLEEP);
        unsigned mx = 0;
#pragma unroll
        for (int k = 0; k < 10; ++k) mv[2 * k] = __builtin_amdgcn_raw_buffer_load_b128(rm, s * 10240 + k * 1024 + moff2, 0, 16);
#pragma unroll
        for (int k = 0; k < 10; ++k) mx = umax4(mx, mv[2 * k]);
        if (!__any(mx == 0xFFFFFFFFu)) break;
        if (++spins > SPIN_CAP) { __hip_atomic_store(tmo, 1u, __ATOMIC_RELAXED, __HIP_MEMORY_SCOPE_AGENT); break; }
      }
      pre_m = nextpre<PRE_U>(t0, wmin_m);
#pragma unroll
      for (int k = 0; k < 10; ++k) mv[2 * k + 1] = ror8(mv[2 * k]);
    }
    STAMP(1);
    {
      f32x4 acc[4];
#pragma unroll
      for (int nt = 0; nt < 4; ++nt) acc[nt] = (f32x4){0.f, 0.f, 0.f, 0.f};
      asm volatile("s_nop 1" : "+v"(mv[1]), "+v"(mv[3]), "+v"(mv[5]), "+v"(mv[7]), "+v"(mv[9]), "+v"(mv[11]), "+v"(mv[13]), "+v"(mv[15]), "+v"(mv[17]), "+v"(mv[19]));
#pragma unroll
      for (int ks = 0; ks < 20; ++ks) {
        const bf16x8 mf = __builtin_bit_cast(bf16x8, mv[ks]);
        MFMA_BA(acc[0], mf, ba[ks]);
        MFMA_BA(acc[1], mf, ba[20 + ks]);
        MFMA_BA(acc[2], mf, ba[40 + ks]);
        if (ks < 4) MFMA_BA(acc[3], mf, ba[60 + ks]); else MFMA_VV(acc[3], mf, bv[ks - 4]);
      }
      asm volatile("s_nop 7\n\ts_nop 1" : "+v"(acc[0]), "+v"(acc[1]), "+v"(acc[2]), "+v"(acc[3]));
      if (g < 2) {
#pragma unroll
        for (int nt = 0; nt < 4; ++nt) {
          int hh = (4 * w + nt) * 2 + (fr >> 3), cc = fr & 7;
          v2u v = pk4(acc[nt]);
          *(v2u*)(lub + (hh * 8 + cc) * 16 + g * 8) = v;
          __builtin_amdgcn_raw_buffer_store_b64(v, rum, (((s * H_ + hh) * D_ + 8 * u + cc) * 8 + g * 4) * 2, 0, 16);
        }
      }
    }
    STAMP(2);
    __syncthreads();
    if (tid == 64 && s > 0) __hip_atomic_fetch_add(ctl(a.ws, C_S + s - 1), 1u, __ATOMIC_RELAXED, __HIP_MEMORY_SCOPE_AGENT);
#pragma unroll
    for (int k = 0; k < 2; ++k) ub[8 + k] = *(const bf16x8*)(lub + ((4 * (2 * w + k) + g) * 8 + (fr & 7)) * 16);
    STAMP(3);
    v4u pv[10];
    {
      unsigned spins = 0;
      const unsigned long long t0 = __builtin_amdgcn_s_memtime();
      presleep(t0, pre_p);
      for (;;) {
        unsigned mx = 0;
#pragma unroll
        for (int k = 0; k < 4; ++k) pv[2 * k] = __builtin_amdgcn_raw_buffer_load_b128(rp, s * 20480 + (8 * w + 2 * k) * 512 + moff2, 0, 16);
        pv[8] = __builtin_amdgcn_raw_buffer_load_b128(rp, s * 20480 + (32 + 2 * w) * 512 + moff2, 0, 16);
#pragma unroll
        for (int k = 0; k < 5; ++k) mx = umax4(mx, pv[2 * k]);
        if (!__any(mx == 0xFFFFFFFFu)) break;
        if (++spins > SPIN_CAP) { __hip_atomic_store(tmo, 1u, __ATOMIC_RELAXED, __HIP_MEMORY_SCOPE_AGENT); break; }
      }
      pre_p = nextpre(t0, wmin_p);
#pragma unroll
      for (int k = 0; k < 5; ++k) pv[2 * k + 1] = ror8(pv[2 * k]);
    }
    STAMP(4);
    f32x4 acc0 = {0.f, 0.f, 0.f, 0.f}, acc1 = {0.f, 0.f, 0.f, 0.f};
    asm volatile("s_nop 1" : "+v"(acc0), "+v"(acc1), "+v"(pv[1]), "+v"(pv[3]), "+v"(pv[5]), "+v"(pv[7]), "+v"(pv[9]));
#pragma unroll
    for (int k = 0; k < 10; k += 2) {
      MFMA_VV(acc0, ub[k], __builtin_bit_cast(bf16x8, pv[k]));
      MFMA_VV(acc1, ub[k + 1], __builtin_bit_cast(bf16x8, pv[k + 1]));
    }
    asm volatile("s_nop 7\n\ts_nop 1" : "+v"(acc0), "+v"(acc1));
    acc0 += acc1;
    if (fr < 8 && g < 2) *(f32x4*)(red + (w * 8 + fr) * 8 + 4 * g) = acc0;
    __syncthreads();
    STAMP(5);
    if (tid < 16) {
      const int i = tid >> 1, hf = tid & 1;
      f32x4 o = *(const f32x4*)(red + (0 * 8 + i) * 8 + 4 * hf);
#pragma unroll
      for (int ww = 1; ww < 4; ++ww) o += *(const f32x4*)(red + (ww * 8 + i) * 8 + 4 * hf);
      v2u v = {canon2(pack2(o[0], o[1])), canon2(pack2(o[2], o[3]))};
      __builtin_amdgcn_raw_buffer_store_b64(v, rm, (s + 1) * 10240 + u * 128 + tid * 8, 0, 16);
    }
    STAMP(6);
  }
  publish(ctl(a.ws, C_S + NSEG_ - 1));
}

#define QKV_COMPUTE(BASE, KN) do { \
    char* base_ = (BASE); \
    bf16x8 af[4], bf[4]; \
    _Pragma("unroll") for (int i = 0; i < 4; ++i) { int row = wm * 64 + i * 16 + fr; af[i] = *(const bf16x8*)(base_ + row * 64 + ((g ^ (((row >> 3) & 1) * 3)) * 16)); } \
    _Pragma("unroll") for (int j = 0; j < 4; ++j) { const int nt = wn * NTW + j; bf[j] = tr_frag(base_ + 8192 + (nt >> 2) * 4096, 0, (nt & 3) * 2, lane); } \
    _Pragma("unroll") for (int j = 0; j < NTW; ++j) { \
      __builtin_amdgcn_sched_barrier(0); \
      _Pragma("unroll") for (int i = 0; i < 4; ++i) { if (i < 3 || j < 10) MFMA_ACCA(acc[i][j], bf[j & 3], af[i]); else MFMA_ACCV(acc[i][j], bf[j & 3], af[i]); } \
      __builtin_amdgcn_sched_barrier(0); \
      if (j + 4 < NTW) { const int nt = wn * NTW + j + 4; bf[j & 3] = tr_frag(base_ + 8192 + (nt >> 2) * 4096, 0, (nt & 3) * 2, lane); } \
      if ((KN) >= 0) { const int o0 = (j * NOPS + NTW - 1) / NTW, o1 = ((j + 1) * NOPS + NTW - 1) / NTW; if (o1 > o0 && o0 < NOPS) stage_op((KN), o0); } \
    } } while (0)
template <int SUB0, int NS>
__device__ __forceinline__ void task_qkvu(const MegaArgs& a, char* smem, int mt, int h) {
  constexpr int BM = 128, NTW = 2 * NS, NOPS = 2 + NS;
  constexpr bool HASV = (SUB0 + NS == 9);
  const int tid = otid(), lane = tid & 63, wid = tid >> 6, wm = wid >> 1, wn = wid & 1, fr = lane & 15, g = lane >> 4;
  char* vimg = smem + 96 * 1024;
  const unsigned ldsw = __builtin_amdgcn_readfirstlane((unsigned)(size_t)smem + (tid >> 6) * 1024);
  auto wo_stage = [&](int pass) {
#pragma unroll
    for (int it = 0; it < 6; ++it) wo_op_(a, smem, tid, h, pass, it);
  };
  {
    constexpr int STG = 8192 + NS * 4096;
    const u16* asrc = (const u16*)(a.ws + WS_H1) + (size_t)(mt * BM + (tid >> 2)) * D_ + (((tid & 3) ^ (((tid >> 5) & 1) * 3)) * 8);
    const size_t boff = (size_t)(tid >> 3) * HD_ + h * DH_ + (((tid & 7) ^ fswz(tid >> 3)) * 8);
    auto stage_op = [&](int kt, int op) {
      const unsigned base = ldsw + (kt % 3) * STG;
      if (op < 2) glds16(asrc + (size_t)op * 64 * D_ + kt * 32, base + op * 4096);
      else {
        const int it = op - 2, sb = SUB0 + it;
        const u16* W = (sb < 3) ? a.Wq : (sb < 6 ? a.Wk : a.Wv);
        glds16(W + boff + (size_t)(kt * 32) * HD_ + (sb % 3) * 64, base + 8192 + it * 4096);
      }
    };
    f32x4 acc[4][NTW];
#pragma unroll
    for (int i = 0; i < 4; ++i)
#pragma unroll
      for (int j = 0; j < NTW; ++j) acc[i][j] = (f32x4){0.f, 0.f, 0.f, 0.f};
#pragma unroll
    for (int op = 0; op < NOPS; ++op) stage_op(0, op);
#pragma unroll
    for (int op = 0; op < NOPS; ++op) stage_op(1, op);
    for (int kb = 0; kb < 18; kb += 3) {
#pragma unroll
      for (int d = 0; d < 3; ++d) {
        const int kt = kb + d;
        WAITV(NOPS);
        RAWBAR();
        QKV_COMPUTE(smem + d * STG, kt + 2);
      }
    }
    WAITV(NOPS); RAWBAR(); QKV_COMPUTE(smem + 0 * STG, -1);
    WAITV(0); RAWBAR(); QKV_COMPUTE(smem + 1 * STG, -1);
    RAWBAR();
#pragma unroll
    for (int j = 0; j < NTW; ++j) asm volatile("s_nop 7\n\ts_nop 1" : "+a"(acc[0][j]), "+a"(acc[1][j]), "+a"(acc[2][j]));
#pragma unroll
    for (int j = 0; j < NTW; ++j) { if (j < 10) asm volatile("s_nop 7\n\ts_nop 1" : "+a"(acc[3][j])); else asm volatile("s_nop 7\n\ts_nop 1" : "+v"(acc[3][j])); }
    TSTAMP(mt * 32 + h, 3);
    if (HASV) { wo_stage(0); wo_stage(1); wo_stage(2); }
    const __amdgpu_buffer_rsrc_t rq = __builtin_amdgcn_make_buffer_rsrc(a.ws + WS_Q, 0, 2 * S_ * HD_ * 2, 0x00020000);
#pragma unroll
    for (int j = 0; j < NTW; j += 2) {
      const int nt = SUB0 * 4 + wn * NTW + j;
      const int mat = nt / 12, nl = nt - mat * 12;
#pragma unroll
      for (int i = 0; i < 4; ++i) {
        if (mat < 2) {
          v4u v = pair16(pk4(acc[i][j]), pk4(acc[i][j + 1]));
          int tok = mt * BM + wm * 64 + i * 16 + fr, n = h * DH_ + (nl + (g & 1)) * 16 + (g >> 1) * 8;
          __builtin_amdgcn_raw_buffer_store_b128(v, rq, mat * (S_ * HD_ * 2) + (tok * HD_ + n) * 2, 0, 16);
        } else if (HASV) {
#pragma unroll
          for (int jj = 0; jj < 2; ++jj) {
            int row = wm * 64 + i * 16 + fr, n = (nl + jj) * 16 + 4 * g;
            *(v2u*)(vimg + (n >> 6) * 16384 + row * 128 + ((((n >> 3) & 7) ^ (row & 7)) * 16) + ((n >> 2) & 1) * 8) = pk4(acc[i][j + jj]);
          }
        }
      }
    }
  }
  if (HASV) u_gemm<(NS == 9 ? 36 : 0), (NS == 9 ? 12 : 0)>(a, smem, tid, mt * BM, h);
}

__device__ __forceinline__ void task_x(const MegaArgs& a, char* smem, int s, int hq) {
  const int tid = otid(), lane = tid & 63, w = tid >> 6, fr = lane & 15, g = lane >> 4;
  const u16* Q = (const u16*)(a.ws + WS_Q);
  const u16* Kx = (const u16*)(a.ws + WS_K);
  const u16* km = (const u16*)(a.ws + WS_KM);
  const u16* U = (const u16*)(a.ws + WS_U);
  const u16* Um = (const u16*)(a.ws + WS_UM);
  constexpr int PSTR = 336;
  const float scale = 0.07216878364870322f;
  bf16x8 uf[5][10];
  bf16x8 kfa[6][3], qfa[6][2];
#pragma unroll
  for (int ks = 0; ks < 6; ++ks) {
    const int eo = (4 * hq + w) * DH_ + ks * 32 + 8 * g;
    kfa[ks][0] = *(const bf16x8*)(km + ((size_t)s * 8 + (fr & 7)) * HD_ + eo);
    kfa[ks][1] = *(const bf16x8*)(Kx + (size_t)(s * 32 + fr) * HD_ + eo);
    kfa[ks][2] = *(const bf16x8*)(Kx + (size_t)(s * 32 + 16 + fr) * HD_ + eo);
    qfa[ks][0] = *(const bf16x8*)(Q + (size_t)(s * 32 + fr) * HD_ + eo);
    qfa[ks][1] = *(const bf16x8*)(Q + (size_t)(s * 32 + 16 + fr) * HD_ + eo);
  }
  auto uload = [&](int ks) {
    const int kc = ks * 4 + g, hl = kc / 5, jc = kc - hl * 5, h = 4 * hq + hl;
    const u16* ub = ((jc == 0) ? Um + (((size_t)s * H_ + h) * D_) * 8 : U + (((size_t)h * UCH_ + s * 4 + jc - 1) * D_) * 8) + (size_t)(160 * w + fr) * 8;
#pragma unroll
    for (int ct = 0; ct < 10; ++ct) uf[ks][ct] = *(const bf16x8*)(ub + ct * 128);
  };
  uload(0); uload(1);
  __builtin_amdgcn_sched_barrier(0);
  {
    f32x4 acc[3][2];
#pragma unroll
    for (int kt = 0; kt < 3; ++kt) { acc[kt][0] = (f32x4){0.f, 0.f, 0.f, 0.f}; acc[kt][1] = (f32x4){0.f, 0.f, 0.f, 0.f}; }
#pragma unroll
    for (int ks = 0; ks < 6; ++ks)
#pragma unroll
      for (int kt = 0; kt < 3; ++kt)
#pragma unroll
        for (int tt = 0; tt < 2; ++tt) acc[kt][tt] = __builtin_amdgcn_mfma_f32_16x16x32_bf16(kfa[ks][kt], qfa[ks][tt], acc[kt][tt], 0, 0, 0);
    __builtin_amdgcn_sched_barrier(0);
    uload(2); uload(3); uload(4);
    __builtin_amdgcn_sched_barrier(0);
#pragma unroll
    for (int tt = 0; tt < 2; ++tt) {
      const int t = tt * 16 + fr;
      float sc[12]; bool ok[12];
      float mx = -3.0e38f;
#pragma unroll
      for (int kt = 0; kt < 3; ++kt)
#pragma unroll
        for (int r = 0; r < 4; ++r) {
          int jl = 4 * g + r;
          bool v = (kt == 0) ? (jl < 8) : ((kt - 1) * 16 + jl <= t);
          float xx = rbf(rbf(acc[kt][tt][r]) * scale);
          sc[kt * 4 + r] = xx; ok[kt * 4 + r] = v;
          if (v) mx = fmaxf(mx, xx);
        }
      mx = xmax16(mx); mx = xmax32(mx);
      float sum = 0.f;
#pragma unroll
      for (int e = 0; e < 12; ++e) { sc[e] = ok[e] ? __expf(sc[e] - mx) : 0.f; sum += sc[e]; }
      sum = xadd16(sum); sum = xadd32(sum);
      const float inv = 1.f / sum;
      char* dst = smem + t * PSTR + w * 80;
      if (g < 2) *(v2u*)(dst + 8 * g) = (v2u){pack2(sc[0] * inv, sc[1] * inv), pack2(sc[2] * inv, sc[3] * inv)};
      *(v2u*)(dst + 16 + 8 * g) = (v2u){pack2(sc[4] * inv, sc[5] * inv), pack2(sc[6] * inv, sc[7] * inv)};
      *(v2u*)(dst + 48 + 8 * g) = (v2u){pack2(sc[8] * inv, sc[9] * inv), pack2(sc[10] * inv, sc[11] * inv)};
    }
  }
  __syncthreads();
  f32x4 acc[10][2];
#pragma unroll
  for (int ct = 0; ct < 10; ++ct) { acc[ct][0] = (f32x4){0.f, 0.f, 0.f, 0.f}; acc[ct][1] = (f32x4){0.f, 0.f, 0.f, 0.f}; }
#pragma unroll
  for (int ks = 0; ks < 5; ++ks) {
    bf16x8 pf[2];
    pf[0] = *(const bf16x8*)(smem + fr * PSTR + (ks * 4 + g) * 16);
    pf[1] = *(const bf16x8*)(smem + (16 + fr) * PSTR + (ks * 4 + g) * 16);
#pragma unroll
    for (int ct = 0; ct < 10; ++ct)
#pragma unroll
      for (int tt = 0; tt < 2; ++tt) acc[ct][tt] = __builtin_amdgcn_mfma_f32_16x16x32_bf16(uf[ks][ct], pf[tt], acc[ct][tt], 0, 0, 0);
  }
  const __amdgpu_buffer_rsrc_t rx = __builtin_amdgcn_make_buffer_rsrc(a.ws + WS_XP, 0, NSEG_ * 8 * 32 * D_ * 4, 0x00020000);
#pragma unroll
  for (int ct = 0; ct < 10; ++ct)
#pragma unroll
    for (int tt = 0; tt < 2; ++tt) {
      const int t = tt * 16 + fr, c = 160 * w + ct * 16 + 4 * g;
      __builtin_amdgcn_raw_buffer_store_b128(__builtin_bit_cast(v4u, acc[ct][tt]), rx, (((s * 8 + hq) * 32 + t) * D_ + c) * 4, 0, 16);
    }
}

__device__ __forceinline__ void task_xc(const MegaArgs& a, char* smem, int s, int q8) {
  const int tid = otid(), lane = tid & 63, w = tid >> 6;
  const float* mod = (const float*)(a.ws + WS_MOD);
  const float* xp = (const float*)(a.ws + WS_XP) + (size_t)s * 8 * 32 * D_;
  const __amdgpu_buffer_rsrc_t rx1 = __builtin_amdgcn_make_buffer_rsrc(a.ws + WS_X1, 0, S_ * D_ * 2, 0x00020000);
  const __amdgpu_buffer_rsrc_t rh2 = __builtin_amdgcn_make_buffer_rsrc(a.ws + WS_H2, 0, S_ * D_ * 2, 0x00020000);
  float* xl = (float*)smem + w * D_;
  const int t0 = 4 * q8 + w;
  const bool two = lane < 16;
  const int c1 = two ? 512 + lane * 8 : lane * 8;
  f32x4 msb[2][2], mcb[2][2];
  {
    f32x4 pp[2][8][2]; v4u xx[2]; f32x4 gg[2][2];
#pragma unroll
    for (int k = 0; k < 2; ++k) {
      const int c0 = k ? c1 : lane * 8;
#pragma unroll
      for (int pq = 0; pq < 8; ++pq) { pp[k][pq][0] = *(const f32x4*)(xp + ((size_t)pq * 32 + t0) * D_ + c0); pp[k][pq][1] = *(const f32x4*)(xp + ((size_t)pq * 32 + t0) * D_ + c0 + 4); }
      xx[k] = *(const v4u*)(a.x + (size_t)(s * 32 + t0) * D_ + c0);
      gg[k][0] = *(const f32x4*)(mod + 2 * D_ + c0); gg[k][1] = *(const f32x4*)(mod + 2 * D_ + c0 + 4);
      msb[k][0] = *(const f32x4*)(mod + 3 * D_ + c0); msb[k][1] = *(const f32x4*)(mod + 3 * D_ + c0 + 4);
      mcb[k][0] = *(const f32x4*)(mod + 4 * D_ + c0); mcb[k][1] = *(const f32x4*)(mod + 4 * D_ + c0 + 4);
    }
    __builtin_amdgcn_sched_barrier(0);
#pragma unroll
    for (int k = 0; k < 2; ++k) {
      f32x4 s0 = ((pp[k][0][0] + pp[k][1][0]) + (pp[k][2][0] + pp[k][3][0])) + ((pp[k][4][0] + pp[k][5][0]) + (pp[k][6][0] + pp[k][7][0]));
      f32x4 s1 = ((pp[k][0][1] + pp[k][1][1]) + (pp[k][2][1] + pp[k][3][1])) + ((pp[k][4][1] + pp[k][5][1]) + (pp[k][6][1] + pp[k][7][1]));
      f32x4 o0, o1;
#pragma unroll
      for (int e = 0; e < 4; ++e) {
        float x0 = (e & 1) ? bfhi(xx[k][e >> 1]) : bflo(xx[k][e >> 1]);
        float x1v = (e & 1) ? bfhi(xx[k][2 + (e >> 1)]) : bflo(xx[k][2 + (e >> 1)]);
        o0[e] = rbf(x0 + rbf(gg[k][0][e] * rbf(s0[e])));
        o1[e] = rbf(x1v + rbf(gg[k][1][e] * rbf(s1[e])));
      }
      if (k == 0 || two) { const int c0 = k ? c1 : lane * 8; *(f32x4*)(xl + c0) = o0; *(f32x4*)(xl + c0 + 4) = o1; }
    }
  }
#pragma unroll
  for (int rr = 0; rr < 1; ++rr) {
    const int t = t0 + rr;
    float v[16];
    {
      f32x4 p0 = *(const f32x4*)(xl + rr * D_ + lane * 8), p1 = *(const f32x4*)(xl + rr * D_ + lane * 8 + 4);
      f32x4 p2 = *(const f32x4*)(xl + rr * D_ + c1), p3 = *(const f32x4*)(xl + rr * D_ + c1 + 4);
#pragma unroll
      for (int e = 0; e < 4; ++e) { v[e] = p0[e]; v[4 + e] = p1[e]; v[8 + e] = two ? p2[e] : 0.f; v[12 + e] = two ? p3[e] : 0.f; }
    }
    float sm = 0.f;
#pragma unroll
    for (int e = 0; e < 16; ++e) sm += v[e];
    sm = wave_sum(sm);
    const float mean = sm * (1.f / D_);
    float q = 0.f;
#pragma unroll
    for (int e = 0; e < 8; ++e) { float d = v[e] - mean; q += d * d; }
    if (two) {
#pragma unroll
      for (int e = 8; e < 16; ++e) { float d = v[e] - mean; q += d * d; }
    }
    q = wave_sum(q);
    const float mb = rbf(mean), vb = rbf(q * (1.f / D_));
    const float den = rbf(sqrtf(rbf(vb + 1e-5f)));
#pragma unroll
    for (int part = 0; part < 2; ++part) {
      if (part == 0 || two) {
        const int c0 = part * 512 + lane * 8;
        float o[8];
#pragma unroll
        for (int e = 0; e < 8; ++e) {
          float tv = rbf(rbf(v[part * 8 + e] - mb) / den);
          tv = rbf(tv * rbf(1.f + mcb[part][e >> 2][e & 3]));
          o[e] = tv + msb[part][e >> 2][e & 3];
        }
        const int off = ((s * 32 + t) * D_ + c0) * 2;
        v4u hv = {pack2(o[0], o[1]), pack2(o[2], o[3]), pack2(o[4], o[5]), pack2(o[6], o[7])};
        v4u xv = {pack2(v[part * 8 + 0], v[part * 8 + 1]), pack2(v[part * 8 + 2], v[part * 8 + 3]), pack2(v[part * 8 + 4], v[part * 8 + 5]), pack2(v[part * 8 + 6], v[part * 8 + 7])};
        __builtin_amdgcn_raw_buffer_store_b128(hv, rh2, off, 0, 16);
        __builtin_amdgcn_raw_buffer_store_b128(xv, rx1, off, 0, 16);
      }
    }
  }
}

__device__ __forceinline__ void task_f1(const MegaArgs& a, char* smem, int s, int nb) {
  constexpr int BM = 32, BN = 128;
  PGen P; P.A = (const u16*)(a.ws + WS_H2); P.lda = D_; P.W = a.w1; P.W2 = a.w3; P.K = D_; P.ldb = F_;
  const int m0 = s * BM, f0 = nb * 64;
  f32x4 acc[BM / 32][BN / 32];
  gemm_tile_dma<BM, BN, 10, 6, true>(P, m0, f0, smem, acc);
  const int tid = otid(); const int lane = tid & 63, wid = tid >> 6, wm = wid >> 1, wn = wid & 1, fr = lane & 15, g = lane >> 4;
  f32x4* ex = (f32x4*)smem;
  if (wn == 1) {
#pragma unroll
    for (int j = 0; j < BN / 32; ++j) ex[(wm * (BN / 32) + j) * 64 + lane] = acc[0][j];
  }
  __syncthreads();
  if (wn == 0) {
    const __amdgpu_buffer_rsrc_t rg = __builtin_amdgcn_make_buffer_rsrc(a.ws + WS_G, 0, S_ * F_ * 2, 0x00020000);
#pragma unroll
    for (int j = 0; j < BN / 32; j += 2) {
      v2u pk[2];
#pragma unroll
      for (int jj = 0; jj < 2; ++jj) {
        f32x4 up = ex[(wm * (BN / 32) + j + jj) * 64 + lane];
        f32x4 o;
#pragma unroll
        for (int r = 0; r < 4; ++r) {
          float gt = rbf(acc[0][j + jj][r]);
          float sg = rbf(gt / (1.f + __expf(-gt)));
          o[r] = sg * rbf(up[r]);
        }
        pk[jj] = pk4(o);
      }
      v4u v = pair16(pk[0], pk[1]);
      int tok = m0 + wm * (BM / 2) + fr, f = f0 + (j + (g & 1)) * 16 + (g >> 1) * 8;
      __builtin_amdgcn_raw_buffer_store_b128(v, rg, (tok * F_ + f) * 2, 0, 16);
    }
  }
  __syncthreads();
}

__device__ __forceinline__ void task_f2(const MegaArgs& a, char* smem, int s, int nb) {
  constexpr int BM = 32, BN = 64;
  PGen P; P.A = (const u16*)(a.ws + WS_G); P.lda = F_; P.W = a.w2; P.W2 = nullptr; P.K = F_; P.ldb = D_;
  const int m0 = s * BM, n0 = nb * BN;
  f32x4 acc[BM / 32][BN / 32];
  gemm_tile_dma<BM, BN, 24, 8, true>(P, m0, n0, smem, acc);
  const int tid = otid(); const int lane = tid & 63, wid = tid >> 6, wm = wid >> 1, wn = wid & 1, fr = lane & 15, g = lane >> 4;
  const u16* x1 = (const u16*)(a.ws + WS_X1);
  const float* gb = (const float*)(a.ws + WS_MOD) + 5 * D_;
#pragma unroll
  for (int j = 0; j < BN / 32; ++j) {
    int tok = m0 + wm * (BM / 2) + fr, c = n0 + wn * (BN / 2) + j * 16 + 4 * g;
    v2u xv = *(const v2u*)(x1 + (size_t)tok * D_ + c);
    float xo[4] = {bflo(xv[0]), bfhi(xv[0]), bflo(xv[1]), bfhi(xv[1])};
    f32x4 o;
#pragma unroll
    for (int r = 0; r < 4; ++r) o[r] = xo[r] + rbf(gb[c + r] * rbf(acc[0][j][r]));
    *(v2u*)(a.out + (size_t)tok * D_ + c) = pk4(o);
  }
}

constexpr int NP_S = 8 + 24 + 10;
constexpr int D1_ = 8, D2_ = 10;
constexpr int NT2_ = (NSEG_ + D2_) * NP_S;
__device__ __forceinline__ void worker_role(const MegaArgs& a, char* smem, const int x, const int j) {
  unsigned* tmo = ctl(a.ws, C_TMO);
  {
    const int nh = (x == 3) ? 4 : 7, h0 = (x == 3) ? 0 : 4 + (x - 4) * 7, nw = (x == 3) ? 16 : 32;
    const int bq0 = 112 + ((x == 3) ? 0 : 4 + (x - 4) * 11), nbx = (x == 3) ? 4 : 11;
    const int nsplit = 6 * nh + nbx;
    for (int e = j; e < nsplit + (NMT_ - 2) * nh; e += nw) {
      if (e >= 2 * nh && e < 2 * nh + nbx) {
        task_b(a, smem, bq0 + e - 2 * nh);
      } else if (e < nsplit) {
        const int e2 = e < 2 * nh ? e : e - nbx, row = e2 / nh, h = h0 + e2 % nh;
        const int mt = (row == 2 || row == 3 || row == 5) ? 1 : 0;
        if (row == 0 || row == 2) { task_qkvu<6, 3>(a, smem, mt, h); publish(ctl(a.ws, C_K + mt)); }
        else if (row == 1 || row == 3) { task_qkvu<3, 3>(a, smem, mt, h); publish(ctl(a.ws, C_K + mt)); }
        else { task_qkvu<0, 3>(a, smem, mt, h); publish(ctl(a.ws, C_Q0 + mt)); }
      } else {
        const int mt = 2 + (e - nsplit) / nh, h = h0 + (e - nsplit) % nh;
        TSTAMP(mt * 32 + h, 0); TSTAMP(mt * 32 + h, 1);
        task_qkvu<0, 9>(a, smem, mt, h);
        publish(ctl(a.ws, C_K + mt));
        TSTAMP(mt * 32 + h, 2);
      }
    }
  }
  volatile unsigned* lw = (volatile unsigned*)smem;
  for (;;) {
    __syncthreads();
    if (threadIdx.x == 0) lw[0] = __hip_atomic_fetch_add(ctl(a.ws, C_QUEUE), 1u, __ATOMIC_RELAXED, __HIP_MEMORY_SCOPE_AGENT);
    __syncthreads();
    const int t = __builtin_amdgcn_readfirstlane((int)lw[0]);
    __syncthreads();
    if (t >= NT2_) break;
    const int r = t % NP_S;
    const int s = t / NP_S - (r < 8 ? 0 : (r < 32 ? D1_ : D2_));
    if (s < 0 || s >= NSEG_) continue;
    TSTAMP(512 + s * NP_S + r, 0);
    if (r < 8) {
      const int hq = r;
      if (threadIdx.x == 0) { if (s < 8) spin_ge(ctl(a.ws, C_Q0 + (s >> 2)), 32u, tmo); spin_ge(ctl(a.ws, C_K + (s >> 2)), s < 8 ? 64u : 32u, tmo); spin_ge(ctl(a.ws, C_S + s), (unsigned)NCHAIN_, tmo); acquire_all(); }
      __syncthreads();
      TSTAMP(512 + s * NP_S + r, 1);
      task_x(a, smem, s, hq);
      publish(ctl(a.ws, C_T + s));
      if (threadIdx.x == 0) { spin_ge(ctl(a.ws, C_T + s), 8u, tmo); acquire_all(); }
      __syncthreads();
      TSTAMP(512 + s * NP_S + r, 3);
      task_xc(a, smem, s, hq);
      publish(ctl(a.ws, C_XS + s));
    } else if (r < 32) {
      if (threadIdx.x == 0) { spin_ge(ctl(a.ws, C_XS + s), 8u, tmo); acquire_all(); }
      __syncthreads();
      TSTAMP(512 + s * NP_S + r, 1);
      task_f1(a, smem, s, r - 8);
      publish(ctl(a.ws, C_FS + s));
    } else {
      if (threadIdx.x == 0) { spin_ge(ctl(a.ws, C_FS + s), 24u, tmo); acquire_all(); }
      __syncthreads();
      TSTAMP(512 + s * NP_S + r, 1);
      task_f2(a, smem, s, r - 32);
      __syncthreads();
    }
    TSTAMP(512 + s * NP_S + r, 2);
  }
}

__global__ void __launch_bounds__(256, 1) k_mega(MegaArgs a) {
  extern __shared__ __attribute__((aligned(16))) char smem[];
  const int x = blockIdx.x & 7, j = blockIdx.x >> 3;
  if (x < 3 || (x == 3 && j < 16)) {
    const int cidx = (x < 3) ? x * 32 + j : 96 + j;
    if (cidx < H_) score_role(a, smem, cidx);
    else u_role(a, smem, cidx);
  } else {
    worker_role(a, smem, x, x == 3 ? j - 16 : j);
  }
}

extern "C" void kernel_launch(void* const* d_in, const int* in_sizes, int n_in,
                              void* d_out, int out_size, void* d_ws, size_t ws_size,
                              hipStream_t stream) {
  const u16* x = (const u16*)d_in[0];
  const u16* mem0 = (const u16*)d_in[1];
  const u16* Wq = (const u16*)d_in[2];
  const u16* Wk = (const u16*)d_in[3];
  const u16* Wv = (const u16*)d_in[4];
  const u16* Wo = (const u16*)d_in[5];
  const u16* w1 = (const u16*)d_in[8];
  const u16* w3 = (const u16*)d_in[9];
  const u16* w2 = (const u16*)d_in[10];
  const u16* c = (const u16*)d_in[11];
  const u16* wmod = (const u16*)d_in[12];
  char* ws = (char*)d_ws;
  float* mod = (float*)(ws + WS_MOD);

  (void)hipFuncSetAttribute((const void*)k_mega, hipFuncAttributeMaxDynamicSharedMemorySize, MEGA_LDS);

  k_pre<<<240, 256, 0, stream>>>(c, wmod, mem0, ws);
  k_ln<<<S_ / 4, 256, 0, stream>>>(x, mod + 0 * D_, mod + 1 * D_, (u16*)(ws + WS_H1));
  MegaArgs ma; ma.x = x; ma.Wq = Wq; ma.Wk = Wk; ma.Wv = Wv; ma.Wo = Wo; ma.w1 = w1; ma.w3 = w3; ma.w2 = w2; ma.ws = ws; ma.out = (u16*)d_out;
  k_mega<<<NBLK_, 256, MEGA_LDS, stream>>>(ma);
}
```

```cpp
#include <hip/hip_runtime.h>
#include <hip/hip_bf16.h>
#include <stdint.h>

constexpr int S_ = 2048, D_ = 640, H_ = 32, DH_ = 192, NSEG_ = 64, F_ = 1536;
constexpr int HD_ = H_ * DH_;
constexpr int NU_ = 80;
constexpr int UROWS_ = S_ + D_;
constexpr int UCH_ = UROWS_ / 8;
constexpr int NCHAIN_ = H_ + NU_;
constexpr int NBLK_ = 256;
constexpr int NWRK_ = NBLK_ - NCHAIN_;
constexpr int NMT_ = 16;

typedef short bf16x8 __attribute__((ext_vector_type(8)));
typedef short bf16x4 __attribute__((ext_vector_type(4)));
typedef float f32x4 __attribute__((ext_vector_type(4)));
typedef unsigned v4u __attribute__((ext_vector_type(4)));
typedef unsigned v2u __attribute__((ext_vector_type(2)));
typedef unsigned short u16;
typedef short v4i16_t __attribute__((ext_vector_type(4)));

__device__ __forceinline__ float bf2f(u16 x) { return __uint_as_float(((unsigned)x) << 16); }
__device__ __forceinline__ float bflo(unsigned x) { return __uint_as_float(x << 16); }
__device__ __forceinline__ float bfhi(unsigned x) { return __uint_as_float(x & 0xFFFF0000u); }
__device__ __forceinline__ u16 f2bf(float f) { return __builtin_bit_cast(u16, (__bf16)f); }
__device__ __forceinline__ float rbf(float f) { return bf2f(f2bf(f)); }
typedef __bf16 bf2_t __attribute__((ext_vector_type(2)));
typedef float f2_t __attribute__((ext_vector_type(2)));
__device__ __forceinline__ unsigned pack2(float a, float b) { f2_t f = {a, b}; bf2_t r = __builtin_convertvector(f, bf2_t); return __builtin_bit_cast(unsigned, r); }
__device__ __forceinline__ unsigned canon2(unsigned x) { return x == 0xFFFFFFFFu ? 0xFFFFFFFEu : x; }
__device__ __forceinline__ unsigned umax4(unsigned m, v4u v) {
  m = max(max(m, v[0]), v[1]);
  return max(max(m, v[2]), v[3]);
}

constexpr size_t al256(size_t x) { return (x + 255) & ~(size_t)255; }
constexpr size_t WS_CTL  = 0;
constexpr size_t CTL_BYTES = 32768;
constexpr size_t WS_MEMB = CTL_BYTES;
constexpr size_t WS_PB   = WS_MEMB + (size_t)65 * 10240;
constexpr size_t WS_SENT_END = WS_PB + (size_t)64 * 20480;
constexpr size_t WS_MOD  = al256(WS_SENT_END);
constexpr size_t WS_H1   = al256(WS_MOD + 3840 * 4);
constexpr size_t WS_Q    = al256(WS_H1 + (size_t)S_ * D_ * 2);
constexpr size_t WS_K    = WS_Q + (size_t)S_ * HD_ * 2;
constexpr size_t WS_U    = WS_K + (size_t)S_ * HD_ * 2;
constexpr size_t WS_WT   = WS_U + (size_t)H_ * UCH_ * D_ * 8 * 2;
constexpr size_t WS_KM   = WS_WT + (size_t)2 * D_ * HD_ * 2;
constexpr size_t WS_UM   = WS_KM + (size_t)NSEG_ * 8 * HD_ * 2;
constexpr size_t WS_X1   = WS_UM + (size_t)NSEG_ * H_ * D_ * 8 * 2;
constexpr size_t WS_H2   = WS_X1 + (size_t)S_ * D_ * 2;
constexpr size_t WS_G    = WS_H2 + (size_t)S_ * D_ * 2;
constexpr size_t WS_XP   = WS_G + (size_t)S_ * F_ * 2;
constexpr size_t WS_END  = WS_XP + (size_t)NSEG_ * 8 * 32 * D_ * 4;
constexpr int C_TMO = 0, C_K = 1, C_X = 49, C_F = 65, C_S = 81, C_T = 145, C_QUEUE = 210, C_XS = 256, C_FS = 320, C_Q0 = 211, C_B = 213;
__device__ __forceinline__ unsigned* ctl(char* ws, int idx) { return (unsigned*)(ws + WS_CTL) + idx * 16; }

constexpr unsigned SPIN_CAP = 1u << 22;
#define SPIN_SLEEP 12

__device__ __forceinline__ void spin_ge(unsigned* c, unsigned target, unsigned* tmo) {
  unsigned spins = 0;
  while (__hip_atomic_load(c, __ATOMIC_RELAXED, __HIP_MEMORY_SCOPE_AGENT) < target) {
    __builtin_amdgcn_s_sleep(SPIN_SLEEP);
    if (++spins > SPIN_CAP) { __hip_atomic_store(tmo, 1u, __ATOMIC_RELAXED, __HIP_MEMORY_SCOPE_AGENT); break; }
  }
}
__device__ __forceinline__ void acquire_all() {
  __builtin_amdgcn_fence(__ATOMIC_ACQUIRE, "agent");
  asm volatile("s_waitcnt vmcnt(0)" ::: "memory");
}
__device__ __forceinline__ void publish(unsigned* c) {
  asm volatile("s_waitcnt vmcnt(0)" ::: "memory");
  __syncthreads();
  if (threadIdx.x == 64) __hip_atomic_fetch_add(c, 1u, __ATOMIC_RELAXED, __HIP_MEMORY_SCOPE_AGENT);
  __syncthreads();
}

__device__ __forceinline__ v4u pair16(v2u A, v2u B) {
  auto r0 = __builtin_amdgcn_permlane16_swap(A[0], B[0], false, false);
  auto r1 = __builtin_amdgcn_permlane16_swap(A[1], B[1], false, false);
  unsigned a0 = r0[0], b0 = r0[1], a1 = r1[0], b1 = r1[1];
  return (v4u){a0, a1, b0, b1};
}
__device__ __forceinline__ int otid() { int t = threadIdx.x; asm volatile("" : "+v"(t)); return t; }
__device__ __forceinline__ v2u pk4(f32x4 v) { return (v2u){pack2(v[0], v[1]), pack2(v[2], v[3])}; }

__global__ void __launch_bounds__(256) k_ln(const u16* __restrict__ x, const float* __restrict__ shift, const float* __restrict__ scale, u16* __restrict__ y) {
  const int lane = threadIdx.x & 63, w = threadIdx.x >> 6;
  const int row = blockIdx.x * 4 + w;
  const unsigned* xr = (const unsigned*)(x + (size_t)row * D_);
  float v[10];
  float s = 0.f;
#pragma unroll
  for (int i = 0; i < 5; ++i) { unsigned u = xr[lane + 64 * i]; v[2 * i] = bflo(u); v[2 * i + 1] = bfhi(u); s += v[2 * i] + v[2 * i + 1]; }
#pragma unroll
  for (int m = 32; m >= 1; m >>= 1) s += __shfl_xor(s, m);
  const float mean = s * (1.f / D_);
  float q = 0.f;
#pragma unroll
  for (int i = 0; i < 10; ++i) { float d = v[i] - mean; q += d * d; }
#pragma unroll
  for (int m = 32; m >= 1; m >>= 1) q += __shfl_xor(q, m);
  const float mb = rbf(mean), vb = rbf(q * (1.f / D_));
  const float den = rbf(sqrtf(rbf(vb + 1e-5f)));
  unsigned* yr = (unsigned*)(y + (size_t)row * D_);
#pragma unroll
  for (int i = 0; i < 5; ++i) {
    int c = 2 * (lane + 64 * i);
    float o[2];
#pragma unroll
    for (int e = 0; e < 2; ++e) {
      float t = rbf(rbf(v[2 * i + e] - mb) / den);
      t = rbf(t * rbf(1.f + scale[c + e]));
      o[e] = t + shift[c + e];
    }
    yr[lane + 64 * i] = pack2(o[0], o[1]);
  }
}

__device__ __forceinline__ int fswz(int r) { return (((r >> 1) & 1) << 1) | (((r >> 3) & 1) << 2); }

__device__ __forceinline__ bf16x8 tr_frag(const char* sub_base, int krow0, int c0, int lane) {
  const int g = lane >> 4, q = (lane & 15) >> 2, pp = lane & 3;
  v4i16_t lo, hi;
  { int r = krow0 + 8 * g + q; lo = __builtin_amdgcn_ds_read_tr16_b64_v4i16((__attribute__((address_space(3))) v4i16_t*)(sub_base + r * 128 + (((c0 + (pp >> 1)) ^ fswz(r)) * 16) + 8 * (pp & 1))); }
  { int r = krow0 + 8 * g + 4 + q; hi = __builtin_amdgcn_ds_read_tr16_b64_v4i16((__attribute__((address_space(3))) v4i16_t*)(sub_base + r * 128 + (((c0 + (pp >> 1)) ^ fswz(r)) * 16) + 8 * (pp & 1))); }
  return (bf16x8){lo[0], lo[1], lo[2], lo[3], hi[0], hi[1], hi[2], hi[3]};
}

__device__ __forceinline__ void glds16(const void* gsrc, unsigned lds_dst) {
  unsigned keep;
  asm volatile("s_mov_b32 %0, m0\n\ts_mov_b32 m0, %2\n\ts_nop 0\n\tglobal_load_lds_dwordx4 %1, off\n\ts_mov_b32 m0, %0"
               : "=&s"(keep) : "v"(gsrc), "s"(lds_dst) : "memory");
}
#define WAITV(n) asm volatile("s_waitcnt vmcnt(%0)" :: "n"(n) : "memory")
#define RAWBAR() do { asm volatile("s_waitcnt lgkmcnt(0)" ::: "memory"); __builtin_amdgcn_s_barrier(); asm volatile("" ::: "memory"); } while (0)
template <int BM, int BN, int NK, int NBUF, bool SWAP, class PT>
__device__ __forceinline__ void gemm_tile_dma(const PT& P, int m0, int n0, char* smem, f32x4 (&acc)[BM / 32][BN / 32]) {
  constexpr int ABYTES = BM * 128, BBYTES = BN * 128, STG = ABYTES + BBYTES;
  constexpr int NA = BM * 8 / 256, NB = BN * 8 / 256, NOP = NA + NB;
  const int tid = otid(), lane = tid & 63, wid = tid >> 6, wm = wid >> 1, wn = wid & 1;
  const u16* asrc[NA];
  const u16* bsrc[NB];
#pragma unroll
  for (int it = 0; it < NA; ++it) { int p = it * 256 + tid; int row = p >> 3, slot = p & 7; asrc[it] = P.a_row(m0 + row) + ((slot ^ (row & 7)) * 8); }
#pragma unroll
  for (int it = 0; it < NB; ++it) { int p = it * 256 + tid; int sub = p >> 9, r = (p >> 3) & 63, slot = p & 7; bsrc[it] = P.b_col(n0, sub) + (size_t)r * P.ldb + ((slot ^ fswz(r)) * 8); }
  const unsigned ldsw = __builtin_amdgcn_readfirstlane((unsigned)(size_t)smem + (tid >> 6) * 1024);
  auto stage_op = [&](int kt, int op) {
    const unsigned base = ldsw + (kt % NBUF) * STG;
    if (op < NA) glds16(asrc[op] + kt * 64, base + op * 4096);
    else glds16(bsrc[op - NA] + (size_t)kt * 64 * P.ldb, base + ABYTES + (op - NA) * 4096);
  };
#pragma unroll
  for (int i = 0; i < BM / 32; ++i)
#pragma unroll
    for (int j = 0; j < BN / 32; ++j) acc[i][j] = (f32x4){0.f, 0.f, 0.f, 0.f};
  const int fr = lane & 15, g = lane >> 4;
#pragma unroll
  for (int d = 0; d < NBUF - 1; ++d)
#pragma unroll
    for (int op = 0; op < NOP; ++op) stage_op(d, op);
  constexpr int NI = BM / 32, NJ = BN / 32, NM = 2 * NI * NJ;
  constexpr int EVERY = NM / NOP;
#pragma unroll
  for (int kt = 0; kt < NK; ++kt) {
    const int after = (kt + NBUF - 2 < NK - 1 ? NBUF - 2 : NK - 1 - kt);
    if (after == 0) WAITV(0); else if (after == 1) WAITV(NOP); else if (after == 2) WAITV(2 * NOP); else if (after == 3) WAITV(3 * NOP); else if (after == 4) WAITV(4 * NOP); else if (after == 5) WAITV(5 * NOP); else WAITV(6 * NOP);
    RAWBAR();
    char* base = smem + (kt % NBUF) * STG;
    char* bbase = base + ABYTES;
    bf16x8 af[2][NI], bf[2][NJ];
#pragma unroll
    for (int kk = 0; kk < 2; ++kk) {
#pragma unroll
      for (int j = 0; j < NJ; ++j) {
        int col = wn * (BN / 2) + j * 16;
        bf[kk][j] = tr_frag(bbase + (col >> 6) * 8192, kk * 32, (col & 63) >> 3, lane);
      }
#pragma unroll
      for (int i = 0; i < NI; ++i) {
        int row = wm * (BM / 2) + i * 16 + fr;
        af[kk][i] = *(const bf16x8*)(base + row * 128 + (((kk * 4 + g) ^ (row & 7)) * 16));
      }
    }
    __builtin_amdgcn_sched_barrier(0);
    if (kt + NBUF - 1 < NK) stage_op(kt + NBUF - 1, 0);
#pragma unroll
    for (int m = 0; m < NM; ++m) {
      const int kk = m / (NI * NJ), i = (m / NJ) % NI, j = m % NJ;
      acc[i][j] = SWAP ? __builtin_amdgcn_mfma_f32_16x16x32_bf16(bf[kk][j], af[kk][i], acc[i][j], 0, 0, 0)
                       : __builtin_amdgcn_mfma_f32_16x16x32_bf16(af[kk][i], bf[kk][j], acc[i][j], 0, 0, 0);
      if ((m + 1) % EVERY == 0 && (m + 1) / EVERY < NOP) {
        __builtin_amdgcn_sched_barrier(0);
        if (kt + NBUF - 1 < NK) stage_op(kt + NBUF - 1, (m + 1) / EVERY);
        __builtin_amdgcn_sched_barrier(0);
      }
    }
    __builtin_amdgcn_sched_barrier(0);
  }
  RAWBAR();
}

struct PGen {
  const u16* A; const u16* W; const u16* W2; int lda; int K; int ldb;
  __device__ const u16* a_row(int m) const { return A + (size_t)m * lda; }
  __device__ const u16* b_col(int n0, int sub) const { return W2 ? (sub ? W2 : W) + n0 : W + n0 + sub * 64; }
};

__global__ void __launch_bounds__(256) k_pre(const u16* __restrict__ c, const u16* __restrict__ wmod, const u16* __restrict__ mem0, char* ws) {
  __shared__ float red[16][17];
  __shared__ float sv[D_];
  const int b = blockIdx.x, tid = threadIdx.x;
  for (int k = tid; k < D_; k += 256) { float cv = bf2f(c[k]); sv[k] = rbf(cv / (1.f + __expf(-cv))); }
  if (b == 0) {
    u16* memb = (u16*)(ws + WS_MEMB);
    for (int gid = tid; gid < 640; gid += 256) { int u = gid >> 3, i = gid & 7; *(v4u*)(memb + gid * 8) = *(const v4u*)(mem0 + i * D_ + u * 8); }
  }
  {
    const v4u z = {0u, 0u, 0u, 0u}, f = {0xFFFFFFFFu, 0xFFFFFFFFu, 0xFFFFFFFFu, 0xFFFFFFFFu};
    for (size_t o = (size_t)(b * 256 + tid) * 16; o < CTL_BYTES; o += (size_t)240 * 256 * 16) *(v4u*)(ws + WS_CTL + o) = z;
    for (size_t o = (size_t)(b * 256 + tid) * 16; o < WS_SENT_END - WS_MEMB - 10240; o += (size_t)240 * 256 * 16) *(v4u*)(ws + WS_MEMB + 10240 + o) = f;
  }
  __syncthreads();
  const int col = tid & 15, kg = tid >> 4, n = b * 16 + col;
  float acc = 0.f;
#pragma unroll
  for (int k = 0; k < 40; ++k) acc += sv[kg * 40 + k] * bf2f(wmod[(size_t)(kg * 40 + k) * 3840 + n]);
  red[kg][col] = acc;
  __syncthreads();
  if (tid < 16) {
    float t = 0.f;
#pragma unroll
    for (int q = 0; q < 16; ++q) t += red[q][tid];
    ((float*)(ws + WS_MOD))[b * 16 + tid] = rbf(t);
  }
}

struct MegaArgs {
  const u16* x; const u16* Wq; const u16* Wk; const u16* Wv; const u16* Wo; const u16* w1; const u16* w3; const u16* w2;
  char* ws; u16* out;
};
constexpr int MEGA_LDS = 160 * 1024;
#define USLEEP 8
#define STAMP(id)
#define TSTAMP(t, id)
#define MFMA_AA(acc, wa, mb) asm volatile("v_mfma_f32_16x16x32_bf16 %0, %1, %2, %0" : "+v"(acc) : "a"(wa), "v"(mb))
#define MFMA_VV(acc, wa, mb) asm volatile("v_mfma_f32_16x16x32_bf16 %0, %1, %2, %0" : "+v"(acc) : "v"(wa), "v"(mb))
#define MFMA_BA(acc, ma, wb) asm volatile("v_mfma_f32_16x16x32_bf16 %0, %1, %2, %0" : "+v"(acc) : "v"(ma), "a"(wb))
#define MFMA_ACCA(acc, ma, wb) asm("v_mfma_f32_16x16x32_bf16 %0, %1, %2, %0" : "+a"(acc) : "v"(ma), "v"(wb))
#define MFMA_ACCV(acc, ma, wb) asm("v_mfma_f32_16x16x32_bf16 %0, %1, %2, %0" : "+v"(acc) : "v"(ma), "v"(wb))
#define MFMA16(acc, ma, wb) asm volatile("v_mfma_f32_16x16x16_bf16 %0, %1, %2, %0" : "+v"(acc) : "v"(ma), "v"(wb))

#define THROTTLE 0
#define PRE_U 3
#define PSLEEP 0
#define SSLEEP 0
#define PRE_P 3
#define PRE_NUM 3
__device__ __forceinline__ void presleep(unsigned long long t0, unsigned pre) {
  while ((unsigned)(__builtin_amdgcn_s_memtime() - t0) < pre) __builtin_amdgcn_s_sleep(2);
}
template <int NUM = PRE_NUM>
__device__ __forceinline__ unsigned nextpre(unsigned long long t0, unsigned& wmin) {
  unsigned wdur = (unsigned)(__builtin_amdgcn_s_memtime() - t0);
  wmin = min(wmin, wdur);
  return NUM * (wmin >> 3);
}
__device__ __forceinline__ v4u ror8(v4u v) {
  v4u r;
#pragma unroll
  for (int i = 0; i < 4; ++i) r[i] = (unsigned)__builtin_amdgcn_update_dpp(0, (int)v[i], 0x128, 0xf, 0xf, false);
  return r;
}
__device__ __forceinline__ float xmax8(float x) { return fmaxf(x, __builtin_bit_cast(float, __builtin_amdgcn_update_dpp(0, __builtin_bit_cast(int, x), 0x128, 0xf, 0xf, false))); }
__device__ __forceinline__ float xadd8(float x) { return x + __builtin_bit_cast(float, __builtin_amdgcn_update_dpp(0, __builtin_bit_cast(int, x), 0x128, 0xf, 0xf, false)); }
template <int N> __device__ __forceinline__ float dpp_ror_add(float x) { return x + __builtin_bit_cast(float, __builtin_amdgcn_update_dpp(0, __builtin_bit_cast(int, x), 0x120 + N, 0xf, 0xf, false)); }
__device__ __forceinline__ void swap16(float x, float& a, float& b) { auto r = __builtin_amdgcn_permlane16_swap(__float_as_uint(x), __float_as_uint(x), false, false); unsigned r0 = r[0], r1 = r[1]; a = __uint_as_float(r0); b = __uint_as_float(r1); }
__device__ __forceinline__ void swap32(float x, float& a, float& b) { auto r = __builtin_amdgcn_permlane32_swap(__float_as_uint(x), __float_as_uint(x), false, false); unsigned r0 = r[0], r1 = r[1]; a = __uint_as_float(r0); b = __uint_as_float(r1); }
__device__ __forceinline__ float xmax16(float x) { float a, b; swap16(x, a, b); return fmaxf(a, b); }
__device__ __forceinline__ float xadd16(float x) { float a, b; swap16(x, a, b); return a + b; }
__device__ __forceinline__ float xmax32(float x) { float a, b; swap32(x, a, b); return fmaxf(a, b); }
__device__ __forceinline__ float xadd32(float x) { float a, b; swap32(x, a, b); return a + b; }
__device__ __forceinline__ float wave_sum(float x) { x = dpp_ror_add<8>(x); x = dpp_ror_add<4>(x); x = dpp_ror_add<2>(x); x = dpp_ror_add<1>(x); return xadd32(xadd16(x)); }

__device__ __forceinline__ void wo_op_(const MegaArgs& a, char* smem, int tid, int h, int pass, int it) {
  const u16* wosrc = a.Wo + (size_t)h * DH_ * D_ + (size_t)(tid >> 3) * D_ + (((tid & 7) ^ fswz(tid >> 3)) * 8);
  const unsigned ldsw = __builtin_amdgcn_readfirstlane((unsigned)(size_t)smem + (tid >> 6) * 1024);
  glds16(wosrc + (size_t)it * 32 * D_ + pass * 64, ldsw + (pass & 3) * 24576 + it * 4096);
}
template <int NST0, int NST1>
__device__ __forceinline__ void u_gemm(const MegaArgs& a, char* smem, const int tid, const int urow0, const int h) {
  const int lane = tid & 63, wid = tid >> 6, wm = wid >> 1, wn = wid & 1, fr = lane & 15, g = lane >> 4;
  char* vimg = smem + 96 * 1024;
  const __amdgpu_buffer_rsrc_t ru = __builtin_amdgcn_make_buffer_rsrc(a.ws + WS_U, 0, H_ * UCH_ * D_ * 16, 0x00020000);
#pragma unroll
  for (int pass = 0; pass < 10; ++pass) {
    constexpr int N0[10] = {12, 16, 20, 24, 24, 24, 24, 24, 18, 12};
    if (pass < 3 && NST0 != NST1) { if (wn == 0) WAITV(N0[pass] + NST0); else WAITV(N0[pass] + NST1); } else if (pass < 3) WAITV(N0[pass] + NST0); else WAITV(N0[pass]);
    RAWBAR();
    char* bb = smem + (pass & 3) * 24576;
    f32x4 acc[4][2];
#pragma unroll
    for (int i = 0; i < 4; ++i) { acc[i][0] = (f32x4){0.f, 0.f, 0.f, 0.f}; acc[i][1] = (f32x4){0.f, 0.f, 0.f, 0.f}; }
    bf16x8 af[2][4], bf[2][2];
    auto rdfr = [&](int ks, int sl) {
      const int kt = ks >> 1, kk = ks & 1;
#pragma unroll
      for (int j = 0; j < 2; ++j) bf[sl][j] = tr_frag(bb + kt * 8192, kk * 32, (wn * 32 + j * 16) >> 3, lane);
#pragma unroll
      for (int i = 0; i < 4; ++i) { int row = wm * 64 + i * 16 + fr; af[sl][i] = *(const bf16x8*)(vimg + kt * 16384 + row * 128 + (((kk * 4 + g) ^ (row & 7)) * 16)); }
    };
    rdfr(0, 0);
#pragma unroll
    for (int ks = 0; ks < 6; ++ks) {
      if (ks + 1 < 6) rdfr(ks + 1, (ks + 1) & 1);
      if (pass + 3 < 10) wo_op_(a, smem, tid, h, pass + 3, ks);
      __builtin_amdgcn_sched_barrier(0);
#pragma unroll
      for (int i = 0; i < 4; ++i)
#pragma unroll
        for (int j = 0; j < 2; ++j) acc[i][j] = __builtin_amdgcn_mfma_f32_16x16x32_bf16(af[ks & 1][i], bf[ks & 1][j], acc[i][j], 0, 0, 0);
      __builtin_amdgcn_sched_barrier(0);
    }
#pragma unroll
    for (int i = 0; i < 4; i += 2)
#pragma unroll
      for (int j = 0; j < 2; ++j) {
        v4u v = pair16(pk4(acc[i][j]), pk4(acc[i + 1][j]));
        int row = urow0 + wm * 64 + (i + (g & 1)) * 16 + (g >> 1) * 8, c = pass * 64 + wn * 32 + j * 16 + fr;
        __builtin_amdgcn_raw_buffer_store_b128(v, ru, ((h * UCH_ + (row >> 3)) * D_ + c) * 16, 0, 16);
      }
  }
}

__device__ __forceinline__ void task_b(const MegaArgs& a, char* smem, const int q) {
  const int mb = q % 5, hh = q / 5;
  const int t_ = otid();
  const unsigned ldsw = __builtin_amdgcn_readfirstlane((unsigned)(size_t)smem + (t_ >> 6) * 1024);
#pragma unroll
  for (int it = 0; it < 12; ++it) {
    const int row = (it & 3) * 32 + (t_ >> 3);
    glds16(a.Wv + (size_t)(mb * 128 + row) * HD_ + hh * DH_ + (it >> 2) * 64 + (((t_ & 7) ^ (row & 7)) * 8), ldsw + 96 * 1024 + it * 4096);
  }
#pragma unroll
  for (int ps = 0; ps < 3; ++ps)
#pragma unroll
    for (int it = 0; it < 6; ++it) wo_op_(a, smem, t_, hh, ps, it);
  u_gemm<0, 0>(a, smem, t_, S_ + mb * 128, hh);
  publish(ctl(a.ws, C_B));
}

__device__ __forceinline__ void score_role(const MegaArgs& a, char* smem, const int b) {
  const int tid = threadIdx.x, lane = tid & 63, w = __builtin_amdgcn_readfirstlane(tid >> 6);
  const int fr = lane & 15, g = lane >> 4;
  const __amdgpu_buffer_rsrc_t rm = __builtin_amdgcn_make_buffer_rsrc(a.ws + WS_MEMB, 0, 65 * 10240, 0x00020000);
  const __amdgpu_buffer_rsrc_t rp = __builtin_amdgcn_make_buffer_rsrc(a.ws + WS_PB, 0, 64 * 20480, 0x00020000);
  const __amdgpu_buffer_rsrc_t rkm = __builtin_amdgcn_make_buffer_rsrc(a.ws + WS_KM, 0, NSEG_ * 8 * HD_ * 2, 0x00020000);
  const u16* Kx = (const u16*)(a.ws + WS_K);
  unsigned* tmo = ctl(a.ws, C_TMO);
  const int moff2 = (fr >> 3) * 512 + (g * 8 + (fr & 7)) * 16;
  const int h = b;
  char* lw = smem;
  float* part = (float*)(smem + 144 * 1024);
  char* lmem = smem + 149 * 1024;
  task_b(a, smem, NU_ + h);
  bf16x8 wa[64], wv[20];
  {
    const unsigned ldsw = __builtin_amdgcn_readfirstlane((unsigned)(size_t)smem + w * 1024);
    const size_t boff = (size_t)(tid >> 3) * HD_ + h * DH_ + (((tid & 7) ^ fswz(tid >> 3)) * 8);
    auto stage = [&](int ks) {
#pragma unroll
      for (int it = 0; it < 6; ++it) glds16((it < 3 ? a.Wq : a.Wk) + boff + (size_t)(ks * 32) * HD_ + (it % 3) * 64, ldsw + (ks & 3) * 24576 + it * 4096);
    };
    bf16x8* scr = (bf16x8*)(a.ws + WS_WT) + ((size_t)(h * 4 + w) * 36) * 64 + lane;
    stage(0); stage(1); stage(2);
#pragma unroll
    for (int ks = 0; ks < 20; ++ks) {
      if (ks < 18) WAITV(12); else if (ks == 18) WAITV(6); else WAITV(0);
      RAWBAR();
      if (ks + 3 < 20) {
        stage(ks + 3);
      }
#pragma unroll
      for (int p = 0; p < 6; ++p) {
        const int etg = 3 * w + (p % 3);
        bf16x8 v = tr_frag(smem + (ks & 3) * 24576 + (p / 3) * 12288 + (etg >> 2) * 4096, 0, (etg & 3) * 2, lane);
        const int f = p * 20 + ks;
        if (f < 64) wa[f] = v; else if (f < 84) wv[f - 64] = v; else scr[(f - 84) * 64] = v;
      }
    }
    asm volatile("s_waitcnt vmcnt(0)" ::: "memory");
    __syncthreads();
    for (int f = 0; f < 36; ++f) *(bf16x8*)(lw + (w * 36 + f) * 1024 + lane * 16) = scr[f * 64];
  }
  const char* lwp = lw + w * 36 * 1024 + lane * 16;
  const float scale = 0.07216878364870322f;
  unsigned pre_m = 0, wmin_m = 0xFFFFFFFFu;
  for (int s = 0; s < NSEG_; ++s) {
    if ((s & 3) == 0) {
      if (tid == 0) { spin_ge(ctl(a.ws, C_K + (s >> 2)), s < 8 ? 64u : 32u, tmo); acquire_all(); }
      __syncthreads();
    }
    STAMP(0);
    bf16x4 kx[2][3];
#pragma unroll
    for (int kt = 0; kt < 2; ++kt)
#pragma unroll
      for (int et = 0; et < 3; ++et)
        kx[kt][et] = *(const bf16x4*)(Kx + (size_t)(s * 32 + kt * 16 + fr) * HD_ + h * DH_ + (3 * w + et) * 16 + 4 * g);
    bf16x8 l5[2];
    l5[0] = *(const bf16x8*)(lwp + 16 * 1024);
    l5[1] = *(const bf16x8*)(lwp + 17 * 1024);
    {
      v4u mv[3];
      unsigned spins = 0;
      const unsigned long long t0 = __builtin_amdgcn_s_memtime();
      presleep(t0, pre_m);
      for (;;) {
        __builtin_amdgcn_s_sleep(SSLEEP);
        unsigned mx = 0;
#pragma unroll
        for (int k = 0; k < 3; ++k) { const int c = tid + 256 * k; mv[k] = __builtin_amdgcn_raw_buffer_load_b128(rm, s * 10240 + (c < 640 ? c : tid) * 16, 0, 16); }
#pragma unroll
        for (int k = 0; k < 3; ++k) mx = umax4(mx, mv[k]);
        if (!__any(mx == 0xFFFFFFFFu)) break;
        if (++spins > SPIN_CAP) { __hip_atomic_store(tmo, 1u, __ATOMIC_RELAXED, __HIP_MEMORY_SCOPE_AGENT); break; }
      }
      pre_m = nextpre(t0, wmin_m);
#pragma unroll
      for (int k = 0; k < 3; ++k) { const int c = tid + 256 * k; if (c < 640) *(v4u*)(lmem + c * 16) = mv[k]; }
    }
    __syncthreads();
    STAMP(1);
    f32x4 acc[6];
#pragma unroll
    for (int p = 0; p < 6; ++p) acc[p] = (f32x4){0.f, 0.f, 0.f, 0.f};
    bf16x8 l4[2], mf[3];
    l4[0] = *(const bf16x8*)(lwp + 0 * 1024);
    l4[1] = *(const bf16x8*)(lwp + 1 * 1024);
    const char* lmp = lmem + (g * 8 + (fr & 7)) * 16;
    mf[0] = *(const bf16x8*)(lmp); mf[1] = *(const bf16x8*)(lmp + 512); mf[2] = *(const bf16x8*)(lmp + 1024);
    asm volatile("s_nop 1");
#pragma unroll
    for (int ks = 0; ks < 20; ++ks) {
      const bf16x8 m = mf[ks % 3];
      MFMA_AA(acc[0], wa[ks], m);
      MFMA_AA(acc[1], wa[20 + ks], m);
      MFMA_AA(acc[2], wa[40 + ks], m);
      if (ks < 4) MFMA_AA(acc[3], wa[60 + ks], m); else MFMA_VV(acc[3], wv[ks - 4], m);
      if (ks < 4) MFMA_VV(acc[4], wv[16 + ks], m);
      else { MFMA_VV(acc[4], l4[ks & 1], m); if (ks + 2 < 20) l4[ks & 1] = *(const bf16x8*)(lwp + (ks + 2 - 4) * 1024); }
      MFMA_VV(acc[5], l5[ks & 1], m);
      if (ks + 2 < 20) l5[ks & 1] = *(const bf16x8*)(lwp + (16 + ks + 2) * 1024);
      if (ks + 3 < 20) mf[ks % 3] = *(const bf16x8*)(lmp + (ks + 3) * 512);
    }
    asm volatile("s_nop 7\n\ts_nop 1" : "+v"(acc[0]), "+v"(acc[1]), "+v"(acc[2]), "+v"(acc[3]), "+v"(acc[4]), "+v"(acc[5]));
    STAMP(3);
    bf16x4 qf[3], kf[3];
#pragma unroll
    for (int et = 0; et < 3; ++et) { qf[et] = __builtin_bit_cast(bf16x4, pk4(acc[et])); kf[et] = __builtin_bit_cast(bf16x4, pk4(acc[3 + et])); }
    f32x4 st[3];
#pragma unroll
    for (int kt = 0; kt < 3; ++kt) st[kt] = (f32x4){0.f, 0.f, 0.f, 0.f};
    asm volatile("s_nop 1" : "+v"(qf[0]), "+v"(qf[1]), "+v"(qf[2]), "+v"(kf[0]), "+v"(kf[1]), "+v"(kf[2]), "+v"(st[0]), "+v"(st[1]), "+v"(st[2]));
#pragma unroll
    for (int et = 0; et < 3; ++et) {
      MFMA16(st[0], kf[et], qf[et]);
      MFMA16(st[1], kx[0][et], qf[et]);
      MFMA16(st[2], kx[1][et], qf[et]);
    }
    asm volatile("s_nop 7\n\ts_nop 1" : "+v"(st[0]), "+v"(st[1]), "+v"(st[2]));
    float* pt = part;
    if (fr < 8) {
      if (g < 2) *(f32x4*)(pt + (w * 8 + fr) * 40 + 4 * g) = st[0];
      *(f32x4*)(pt + (w * 8 + fr) * 40 + 8 + 4 * g) = st[1];
      *(f32x4*)(pt + (w * 8 + fr) * 40 + 24 + 4 * g) = st[2];
    }
    STAMP(4);
    __syncthreads();
    STAMP(5);
    if (w == 0) {
      const int i = lane & 7, jc = lane >> 3;
      const int jr = jc < 5 ? jc : 0;
      float sc[8];
      f32x4 pp[8];
#pragma unroll
      for (int ww = 0; ww < 4; ++ww) { pp[2 * ww] = *(const f32x4*)(pt + (ww * 8 + i) * 40 + jr * 8); pp[2 * ww + 1] = *(const f32x4*)(pt + (ww * 8 + i) * 40 + jr * 8 + 4); }
      __builtin_amdgcn_sched_barrier(0);
#pragma unroll
      for (int e = 0; e < 4; ++e) { sc[e] = (pp[0][e] + pp[2][e]) + (pp[4][e] + pp[6][e]); sc[4 + e] = (pp[1][e] + pp[3][e]) + (pp[5][e] + pp[7][e]); }
      float mx = -3.0e38f;
#pragma unroll
      for (int e = 0; e < 8; ++e) { sc[e] = rbf(rbf(sc[e]) * scale); mx = fmaxf(mx, sc[e]); }
      mx = xmax8(mx); mx = xmax16(mx); mx = xmax32(mx);
      float sum = 0.f;
#pragma unroll
      for (int e = 0; e < 8; ++e) { sc[e] = __expf(sc[e] - mx); sum += sc[e]; }
      if (jc >= 5) sum = 0.f;
      sum = xadd8(sum); sum = xadd16(sum); sum = xadd32(sum);
      if (jc < 5) {
        const float inv = 1.f / sum;
        v4u o = {canon2(pack2(sc[0] * inv, sc[1] * inv)), canon2(pack2(sc[2] * inv, sc[3] * inv)), canon2(pack2(sc[4] * inv, sc[5] * inv)), canon2(pack2(sc[6] * inv, sc[7] * inv))};
        const int kc = (jc == 0) ? 128 + h : h * 4 + jc - 1;
        __builtin_amdgcn_raw_buffer_store_b128(o, rp, s * 20480 + kc * 128 + i * 16, 0, 16);
      }
    } else if (tid == 64 && s > 0) {
      __hip_atomic_fetch_add(ctl(a.ws, C_S + s - 1), 1u, __ATOMIC_RELAXED, __HIP_MEMORY_SCOPE_AGENT);
    }
    if (fr < 8) {
#pragma unroll
      for (int et = 0; et < 3; ++et)
        __builtin_amdgcn_raw_buffer_store_b64(__builtin_bit_cast(v2u, kf[et]), rkm, ((s * 8 + fr) * HD_ + h * DH_ + (3 * w + et) * 16 + 4 * g) * 2, 0, 16);
    }
    STAMP(6);
    __syncthreads();
  }
  publish(ctl(a.ws, C_S + NSEG_ - 1));
}

__device__ __forceinline__ void u_role(const MegaArgs& a, char* smem, const int b) {
  const int tid = threadIdx.x, lane = tid & 63, w = __builtin_amdgcn_readfirstlane(tid >> 6);
  const int fr = lane & 15, g = lane >> 4;
  const __amdgpu_buffer_rsrc_t rm = __builtin_amdgcn_make_buffer_rsrc(a.ws + WS_MEMB, 0, 65 * 10240, 0x00020000);
  const __amdgpu_buffer_rsrc_t rp = __builtin_amdgcn_make_buffer_rsrc(a.ws + WS_PB, 0, 64 * 20480, 0x00020000);
  const __amdgpu_buffer_rsrc_t rum = __builtin_amdgcn_make_buffer_rsrc(a.ws + WS_UM, 0, NSEG_ * H_ * D_ * 16, 0x00020000);
  const u16* U = (const u16*)(a.ws + WS_U);
  unsigned* tmo = ctl(a.ws, C_TMO);
  const int moff2 = (fr >> 3) * 512 + (g * 8 + (fr & 7)) * 16;
  const int u = b - H_;
  char* lub = smem;
  float* red = (float*)(smem + 4096);
  char* lmem = smem + 8192;
  task_b(a, smem, u);
  if (tid == 0) { spin_ge(ctl(a.ws, C_B), 160u, tmo); acquire_all(); }
  __syncthreads();
  bf16x8 ba[64], bv[16];
#pragma unroll
  for (int f = 0; f < 80; ++f) {
    const int nt = f / 20, ks = f % 20;
    const int hh = (4 * w + nt) * 2 + (fr >> 3), c = 8 * u + (fr & 7);
    bf16x8 v = *(const bf16x8*)(U + (((size_t)hh * UCH_ + 256 + ks * 4 + g) * D_ + c) * 8);
    if (f < 64) ba[f] = v; else bv[f - 64] = v;
  }
  unsigned pre_m = 0, pre_p = 0, wmin_m = 0xFFFFFFFFu, wmin_p = 0xFFFFFFFFu;
  for (int s = 0; s < NSEG_; ++s) {
    if ((s & 3) == 0) {
      if (tid == 0) { spin_ge(ctl(a.ws, C_K + (s >> 2)), s < 8 ? 64u : 32u, tmo); acquire_all(); }
      __syncthreads();
    }
    STAMP(0);
    bf16x8 ub[10];
#pragma unroll
    for (int k = 0; k < 8; ++k) ub[k] = *(const bf16x8*)(U + (((size_t)(8 * w + k) * UCH_ + s * 4 + g) * D_ + 8 * u + (fr & 7)) * 8);
    {
      v4u mv[3];
      unsigned spins = 0;
      const unsigned long long t0 = __builtin_amdgcn_s_memtime();
      presleep(t0, pre_m);
      for (;;) {
        __builtin_amdgcn_s_sleep(USLEEP);
        unsigned mx = 0;
#pragma unroll
        for (int k = 0; k < 3; ++k) { const int c = tid + 256 * k; mv[k] = __builtin_amdgcn_raw_buffer_load_b128(rm, s * 10240 + (c < 640 ? c : tid) * 16, 0, 16); }
#pragma unroll
        for (int k = 0; k < 3; ++k) mx = umax4(mx, mv[k]);
        if (!__any(mx == 0xFFFFFFFFu)) break;
        if (++spins > SPIN_CAP) { __hip_atomic_store(tmo, 1u, __ATOMIC_RELAXED, __HIP_MEMORY_SCOPE_AGENT); break; }
      }
      pre_m = nextpre<PRE_U>(t0, wmin_m);
#pragma unroll
      for (int k = 0; k < 3; ++k) { const int c = tid + 256 * k; if (c < 640) *(v4u*)(lmem + c * 16) = mv[k]; }
    }
    __syncthreads();
    STAMP(1);
    {
      f32x4 acc[4];
#pragma unroll
      for (int nt = 0; nt < 4; ++nt) acc[nt] = (f32x4){0.f, 0.f, 0.f, 0.f};
      const char* lmp = lmem + (g * 8 + (fr & 7)) * 16;
      bf16x8 mf[3];
      mf[0] = *(const bf16x8*)(lmp); mf[1] = *(const bf16x8*)(lmp + 512); mf[2] = *(const bf16x8*)(lmp + 1024);
      asm volatile("s_nop 1");
#pragma unroll
      for (int ks = 0; ks < 20; ++ks) {
        const bf16x8 m = mf[ks % 3];
        MFMA_BA(acc[0], m, ba[ks]);
        MFMA_BA(acc[1], m, ba[20 + ks]);
        MFMA_BA(acc[2], m, ba[40 + ks]);
        if (ks < 4) MFMA_BA(acc[3], m, ba[60 + ks]); else MFMA_VV(acc[3], m, bv[ks - 4]);
        if (ks + 3 < 20) mf[ks % 3] = *(const bf16x8*)(lmp + (ks + 3) * 512);
      }
      asm volatile("s_nop 7\n\ts_nop 1" : "+v"(acc[0]), "+v"(acc[1]), "+v"(acc[2]), "+v"(acc[3]));
      if (g < 2) {
#pragma unroll
        for (int nt = 0; nt < 4; ++nt) {
          int hh = (4 * w + nt) * 2 + (fr >> 3), cc = fr & 7;
          v2u v = pk4(acc[nt]);
          *(v2u*)(lub + (hh * 8 + cc) * 16 + g * 8) = v;
          __builtin_amdgcn_raw_buffer_store_b64(v, rum, (((s * H_ + hh) * D_ + 8 * u + cc) * 8 + g * 4) * 2, 0, 16);
        }
      }
    }
    STAMP(2);
    __syncthreads();
    if (tid == 64 && s > 0) __hip_atomic_fetch_add(ctl(a.ws, C_S + s - 1), 1u, __ATOMIC_RELAXED, __HIP_MEMORY_SCOPE_AGENT);
#pragma unroll
    for (int k = 0; k < 2; ++k) ub[8 + k] = *(const bf16x8*)(lub + ((4 * (2 * w + k) + g) * 8 + (fr & 7)) * 16);
    STAMP(3);
    v4u pv[10];
    {
      unsigned spins = 0;
      const unsigned long long t0 = __builtin_amdgcn_s_memtime();
      presleep(t0, pre_p);
      for (;;) {
        __builtin_amdgcn_s_sleep(PSLEEP);
        unsigned mx = 0;
#pragma unroll
        for (int k = 0; k < 4; ++k) pv[2 * k] = __builtin_amdgcn_raw_buffer_load_b128(rp, s * 20480 + (8 * w + 2 * k) * 512 + moff2, 0, 16);
        pv[8] = __builtin_amdgcn_raw_buffer_load_b128(rp, s * 20480 + (32 + 2 * w) * 512 + moff2, 0, 16);
#pragma unroll
        for (int k = 0; k < 5; ++k) mx = umax4(mx, pv[2 * k]);
        if (!__any(mx == 0xFFFFFFFFu)) break;
        if (++spins > SPIN_CAP) { __hip_atomic_store(tmo, 1u, __ATOMIC_RELAXED, __HIP_MEMORY_SCOPE_AGENT); break; }
      }
      pre_p = nextpre<PRE_P>(t0, wmin_p);
#pragma unroll
      for (int k = 0; k < 5; ++k) pv[2 * k + 1] = ror8(pv[2 * k]);
    }
    STAMP(4);
    f32x4 acc0 = {0.f, 0.f, 0.f, 0.f}, acc1 = {0.f, 0.f, 0.f, 0.f};
    asm volatile("s_nop 1" : "+v"(acc0), "+v"(acc1), "+v"(pv[1]), "+v"(pv[3]), "+v"(pv[5]), "+v"(pv[7]), "+v"(pv[9]));
#pragma unroll
    for (int k = 0; k < 10; k += 2) {
      MFMA_VV(acc0, ub[k], __builtin_bit_cast(bf16x8, pv[k]));
      MFMA_VV(acc1, ub[k + 1], __builtin_bit_cast(bf16x8, pv[k + 1]));
    }
    asm volatile("s_nop 7\n\ts_nop 1" : "+v"(acc0), "+v"(acc1));
    acc0 += acc1;
    if (fr < 8 && g < 2) *(f32x4*)(red + (w * 8 + fr) * 8 + 4 * g) = acc0;
    __syncthreads();
    STAMP(5);
    if (tid < 16) {
      const int i = tid >> 1, hf = tid & 1;
      f32x4 o = *(const f32x4*)(red + (0 * 8 + i) * 8 + 4 * hf);
#pragma unroll
      for (int ww = 1; ww < 4; ++ww) o += *(const f32x4*)(red + (ww * 8 + i) * 8 + 4 * hf);
      v2u v = {canon2(pack2(o[0], o[1])), canon2(pack2(o[2], o[3]))};
      __builtin_amdgcn_raw_buffer_store_b64(v, rm, (s + 1) * 10240 + u * 128 + tid * 8, 0, 16);
    }
    STAMP(6);
  }
  publish(ctl(a.ws, C_S + NSEG_ - 1));
}

#define QKV_COMPUTE(BASE, KN) do { \
    char* base_ = (BASE); \
    bf16x8 af[4], bf[4]; \
    _Pragma("unroll") for (int i = 0; i < 4; ++i) { int row = wm * 64 + i * 16 + fr; af[i] = *(const bf16x8*)(base_ + row * 64 + ((g ^ (((row >> 3) & 1) * 3)) * 16)); } \
    _Pragma("unroll") for (int j = 0; j < 4; ++j) { const int nt = wn * NTW + j; bf[j] = tr_frag(base_ + 8192 + (nt >> 2) * 4096, 0, (nt & 3) * 2, lane); } \
    _Pragma("unroll") for (int j = 0; j < NTW; ++j) { \
      __builtin_amdgcn_sched_barrier(0); \
      _Pragma("unroll") for (int i = 0; i < 4; ++i) { if (i < 3 || j < 10) MFMA_ACCA(acc[i][j], bf[j & 3], af[i]); else MFMA_ACCV(acc[i][j], bf[j & 3], af[i]); } \
      __builtin_amdgcn_sched_barrier(0); \
      if (j + 4 < NTW) { const int nt = wn * NTW + j + 4; bf[j & 3] = tr_frag(base_ + 8192 + (nt >> 2) * 4096, 0, (nt & 3) * 2, lane); } \
      if ((KN) >= 0) { const int o0 = (j * NOPS + NTW - 1) / NTW, o1 = ((j + 1) * NOPS + NTW - 1) / NTW; if (o1 > o0 && o0 < NOPS) stage_op((KN), o0); } \
    } } while (0)
template <int SUB0, int NS>
__device__ __forceinline__ void task_qkvu(const MegaArgs& a, char* smem, int mt, int h) {
  constexpr int BM = 128, NTW = 2 * NS, NOPS = 2 + NS;
  constexpr bool HASV = (SUB0 + NS == 9);
  const int tid = otid(), lane = tid & 63, wid = tid >> 6, wm = wid >> 1, wn = wid & 1, fr = lane & 15, g = lane >> 4;
  char* vimg = smem + 96 * 1024;
  const unsigned ldsw = __builtin_amdgcn_readfirstlane((unsigned)(size_t)smem + (tid >> 6) * 1024);
  auto wo_stage = [&](int pass) {
#pragma unroll
    for (int it = 0; it < 6; ++it) wo_op_(a, smem, tid, h, pass, it);
  };
  {
    constexpr int STG = 8192 + NS * 4096;
    const u16* asrc = (const u16*)(a.ws + WS_H1) + (size_t)(mt * BM + (tid >> 2)) * D_ + (((tid & 3) ^ (((tid >> 5) & 1) * 3)) * 8);
    const size_t boff = (size_t)(tid >> 3) * HD_ + h * DH_ + (((tid & 7) ^ fswz(tid >> 3)) * 8);
    auto stage_op = [&](int kt, int op) {
      const unsigned base = ldsw + (kt % 3) * STG;
      if (op < 2) glds16(asrc + (size_t)op * 64 * D_ + kt * 32, base + op * 4096);
      else {
        const int it = op - 2, sb = SUB0 + it;
        const u16* W = (sb < 3) ? a.Wq : (sb < 6 ? a.Wk : a.Wv);
        glds16(W + boff + (size_t)(kt * 32) * HD_ + (sb % 3) * 64, base + 8192 + it * 4096);
      }
    };
    f32x4 acc[4][NTW];
#pragma unroll
    for (int i = 0; i < 4; ++i)
#pragma unroll
      for (int j = 0; j < NTW; ++j) acc[i][j] = (f32x4){0.f, 0.f, 0.f, 0.f};
#pragma unroll
    for (int op = 0; op < NOPS; ++op) stage_op(0, op);
#pragma unroll
    for (int op = 0; op < NOPS; ++op) stage_op(1, op);
    for (int kb = 0; kb < 18; kb += 3) {
#pragma unroll
      for (int d = 0; d < 3; ++d) {
        const int kt = kb + d;
        WAITV(NOPS);
        RAWBAR();
        QKV_COMPUTE(smem + d * STG, kt + 2);
      }
    }
    WAITV(NOPS); RAWBAR(); QKV_COMPUTE(smem + 0 * STG, -1);
    WAITV(0); RAWBAR(); QKV_COMPUTE(smem + 1 * STG, -1);
    RAWBAR();
#pragma unroll
    for (int j = 0; j < NTW; ++j) asm volatile("s_nop 7\n\ts_nop 1" : "+a"(acc[0][j]), "+a"(acc[1][j]), "+a"(acc[2][j]));
#pragma unroll
    for (int j = 0; j < NTW; ++j) { if (j < 10) asm volatile("s_nop 7\n\ts_nop 1" : "+a"(acc[3][j])); else asm volatile("s_nop 7\n\ts_nop 1" : "+v"(acc[3][j])); }
    TSTAMP(mt * 32 + h, 3);
    if (HASV) { wo_stage(0); wo_stage(1); wo_stage(2); }
    const __amdgpu_buffer_rsrc_t rq = __builtin_amdgcn_make_buffer_rsrc(a.ws + WS_Q, 0, 2 * S_ * HD_ * 2, 0x00020000);
#pragma unroll
    for (int j = 0; j < NTW; j += 2) {
      const int nt = SUB0 * 4 + wn * NTW + j;
      const int mat = nt / 12, nl = nt - mat * 12;
#pragma unroll
      for (int i = 0; i < 4; ++i) {
        if (mat < 2) {
          v4u v = pair16(pk4(acc[i][j]), pk4(acc[i][j + 1]));
          int tok = mt * BM + wm * 64 + i * 16 + fr, n = h * DH_ + (nl + (g & 1)) * 16 + (g >> 1) * 8;
          __builtin_amdgcn_raw_buffer_store_b128(v, rq, mat * (S_ * HD_ * 2) + (tok * HD_ + n) * 2, 0, 16);
        } else if (HASV) {
#pragma unroll
          for (int jj = 0; jj < 2; ++jj) {
            int row = wm * 64 + i * 16 + fr, n = (nl + jj) * 16 + 4 * g;
            *(v2u*)(vimg + (n >> 6) * 16384 + row * 128 + ((((n >> 3) & 7) ^ (row & 7)) * 16) + ((n >> 2) & 1) * 8) = pk4(acc[i][j + jj]);
          }
        }
      }
    }
  }
  if (HASV) u_gemm<(NS == 9 ? 36 : 0), (NS == 9 ? 12 : 0)>(a, smem, tid, mt * BM, h);
}

__device__ __forceinline__ void task_x(const MegaArgs& a, char* smem, int s, int hq) {
  const int tid = otid(), lane = tid & 63, w = tid >> 6, fr = lane & 15, g = lane >> 4;
  const u16* Q = (const u16*)(a.ws + WS_Q);
  const u16* Kx = (const u16*)(a.ws + WS_K);
  const u16* km = (const u16*)(a.ws + WS_KM);
  const u16* U = (const u16*)(a.ws + WS_U);
  const u16* Um = (const u16*)(a.ws + WS_UM);
  constexpr int PSTR = 336;
  const float scale = 0.07216878364870322f;
  bf16x8 uf[5][10];
  bf16x8 kfa[6][3], qfa[6][2];
#pragma unroll
  for (int ks = 0; ks < 6; ++ks) {
    const int eo = (4 * hq + w) * DH_ + ks * 32 + 8 * g;
    kfa[ks][0] = *(const bf16x8*)(km + ((size_t)s * 8 + (fr & 7)) * HD_ + eo);
    kfa[ks][1] = *(const bf16x8*)(Kx + (size_t)(s * 32 + fr) * HD_ + eo);
    kfa[ks][2] = *(const bf16x8*)(Kx + (size_t)(s * 32 + 16 + fr) * HD_ + eo);
    qfa[ks][0] = *(const bf16x8*)(Q + (size_t)(s * 32 + fr) * HD_ + eo);
    qfa[ks][1] = *(const bf16x8*)(Q + (size_t)(s * 32 + 16 + fr) * HD_ + eo);
  }
  auto uload = [&](int ks) {
    const int kc = ks * 4 + g, hl = kc / 5, jc = kc - hl * 5, h = 4 * hq + hl;
    const u16* ub = ((jc == 0) ? Um + (((size_t)s * H_ + h) * D_) * 8 : U + (((size_t)h * UCH_ + s * 4 + jc - 1) * D_) * 8) + (size_t)(160 * w + fr) * 8;
#pragma unroll
    for (int ct = 0; ct < 10; ++ct) uf[ks][ct] = *(const bf16x8*)(ub + ct * 128);
  };
  uload(0); uload(1);
  __builtin_amdgcn_sched_barrier(0);
  {
    f32x4 acc[3][2];
#pragma unroll
    for (int kt = 0; kt < 3; ++kt) { acc[kt][0] = (f32x4){0.f, 0.f, 0.f, 0.f}; acc[kt][1] = (f32x4){0.f, 0.f, 0.f, 0.f}; }
#pragma unroll
    for (int ks = 0; ks < 6; ++ks)
#pragma unroll
      for (int kt = 0; kt < 3; ++kt)
#pragma unroll
        for (int tt = 0; tt < 2; ++tt) acc[kt][tt] = __builtin_amdgcn_mfma_f32_16x16x32_bf16(kfa[ks][kt], qfa[ks][tt], acc[kt][tt], 0, 0, 0);
    __builtin_amdgcn_sched_barrier(0);
    uload(2); uload(3); uload(4);
    __builtin_amdgcn_sched_barrier(0);
#pragma unroll
    for (int tt = 0; tt < 2; ++tt) {
      const int t = tt * 16 + fr;
      float sc[12]; bool ok[12];
      float mx = -3.0e38f;
#pragma unroll
      for (int kt = 0; kt < 3; ++kt)
#pragma unroll
        for (int r = 0; r < 4; ++r) {
          int jl = 4 * g + r;
          bool v = (kt == 0) ? (jl < 8) : ((kt - 1) * 16 + jl <= t);
          float xx = rbf(rbf(acc[kt][tt][r]) * scale);
          sc[kt * 4 + r] = xx; ok[kt * 4 + r] = v;
          if (v) mx = fmaxf(mx, xx);
        }
      mx = xmax16(mx); mx = xmax32(mx);
      float sum = 0.f;
#pragma unroll
      for (int e = 0; e < 12; ++e) { sc[e] = ok[e] ? __expf(sc[e] - mx) : 0.f; sum += sc[e]; }
      sum = xadd16(sum); sum = xadd32(sum);
      const float inv = 1.f / sum;
      char* dst = smem + t * PSTR + w * 80;
      if (g < 2) *(v2u*)(dst + 8 * g) = (v2u){pack2(sc[0] * inv, sc[1] * inv), pack2(sc[2] * inv, sc[3] * inv)};
      *(v2u*)(dst + 16 + 8 * g) = (v2u){pack2(sc[4] * inv, sc[5] * inv), pack2(sc[6] * inv, sc[7] * inv)};
      *(v2u*)(dst + 48 + 8 * g) = (v2u){pack2(sc[8] * inv, sc[9] * inv), pack2(sc[10] * inv, sc[11] * inv)};
    }
  }
  __syncthreads();
  f32x4 acc[10][2];
#pragma unroll
  for (int ct = 0; ct < 10; ++ct) { acc[ct][0] = (f32x4){0.f, 0.f, 0.f, 0.f}; acc[ct][1] = (f32x4){0.f, 0.f, 0.f, 0.f}; }
#pragma unroll
  for (int ks = 0; ks < 5; ++ks) {
    bf16x8 pf[2];
    pf[0] = *(const bf16x8*)(smem + fr * PSTR + (ks * 4 + g) * 16);
    pf[1] = *(const bf16x8*)(smem + (16 + fr) * PSTR + (ks * 4 + g) * 16);
#pragma unroll
    for (int ct = 0; ct < 10; ++ct)
#pragma unroll
      for (int tt = 0; tt < 2; ++tt) acc[ct][tt] = __builtin_amdgcn_mfma_f32_16x16x32_bf16(uf[ks][ct], pf[tt], acc[ct][tt], 0, 0, 0);
  }
  const __amdgpu_buffer_rsrc_t rx = __builtin_amdgcn_make_buffer_rsrc(a.ws + WS_XP, 0, NSEG_ * 8 * 32 * D_ * 4, 0x00020000);
#pragma unroll
  for (int ct = 0; ct < 10; ++ct)
#pragma unroll
    for (int tt = 0; tt < 2; ++tt) {
      const int t = tt * 16 + fr, c = 160 * w + ct * 16 + 4 * g;
      __builtin_amdgcn_raw_buffer_store_b128(__builtin_bit_cast(v4u, acc[ct][tt]), rx, (((s * 8 + hq) * 32 + t) * D_ + c) * 4, 0, 16);
    }
}

__device__ __forceinline__ void task_xc(const MegaArgs& a, char* smem, int s, int q8) {
  const int tid = otid(), lane = tid & 63, w = tid >> 6;
  const float* mod = (const float*)(a.ws + WS_MOD);
  const float* xp = (const float*)(a.ws + WS_XP) + (size_t)s * 8 * 32 * D_;
  const __amdgpu_buffer_rsrc_t rx1 = __builtin_amdgcn_make_buffer_rsrc(a.ws + WS_X1, 0, S_ * D_ * 2, 0x00020000);
  const __amdgpu_buffer_rsrc_t rh2 = __builtin_amdgcn_make_buffer_rsrc(a.ws + WS_H2, 0, S_ * D_ * 2, 0x00020000);
  float* xl = (float*)smem + w * D_;
  const int t0 = 4 * q8 + w;
  const bool two = lane < 16;
  const int c1 = two ? 512 + lane * 8 : lane * 8;
  f32x4 msb[2][2], mcb[2][2];
  {
    f32x4 pp[2][8][2]; v4u xx[2]; f32x4 gg[2][2];
#pragma unroll
    for (int k = 0; k < 2; ++k) {
      const int c0 = k ? c1 : lane * 8;
#pragma unroll
      for (int pq = 0; pq < 8; ++pq) { pp[k][pq][0] = *(const f32x4*)(xp + ((size_t)pq * 32 + t0) * D_ + c0); pp[k][pq][1] = *(const f32x4*)(xp + ((size_t)pq * 32 + t0) * D_ + c0 + 4); }
      xx[k] = *(const v4u*)(a.x + (size_t)(s * 32 + t0) * D_ + c0);
      gg[k][0] = *(const f32x4*)(mod + 2 * D_ + c0); gg[k][1] = *(const f32x4*)(mod + 2 * D_ + c0 + 4);
      msb[k][0] = *(const f32x4*)(mod + 3 * D_ + c0); msb[k][1] = *(const f32x4*)(mod + 3 * D_ + c0 + 4);
      mcb[k][0] = *(const f32x4*)(mod + 4 * D_ + c0); mcb[k][1] = *(const f32x4*)(mod + 4 * D_ + c0 + 4);
    }
    __builtin_amdgcn_sched_barrier(0);
#pragma unroll
    for (int k = 0; k < 2; ++k) {
      f32x4 s0 = ((pp[k][0][0] + pp[k][1][0]) + (pp[k][2][0] + pp[k][3][0])) + ((pp[k][4][0] + pp[k][5][0]) + (pp[k][6][0] + pp[k][7][0]));
      f32x4 s1 = ((pp[k][0][1] + pp[k][1][1]) + (pp[k][2][1] + pp[k][3][1])) + ((pp[k][4][1] + pp[k][5][1]) + (pp[k][6][1] + pp[k][7][1]));
      f32x4 o0, o1;
#pragma unroll
      for (int e = 0; e < 4; ++e) {
        float x0 = (e & 1) ? bfhi(xx[k][e >> 1]) : bflo(xx[k][e >> 1]);
        float x1v = (e & 1) ? bfhi(xx[k][2 + (e >> 1)]) : bflo(xx[k][2 + (e >> 1)]);
        o0[e] = rbf(x0 + rbf(gg[k][0][e] * rbf(s0[e])));
        o1[e] = rbf(x1v + rbf(gg[k][1][e] * rbf(s1[e])));
      }
      if (k == 0 || two) { const int c0 = k ? c1 : lane * 8; *(f32x4*)(xl + c0) = o0; *(f32x4*)(xl + c0 + 4) = o1; }
    }
  }
#pragma unroll
  for (int rr = 0; rr < 1; ++rr) {
    const int t = t0 + rr;
    float v[16];
    {
      f32x4 p0 = *(const f32x4*)(xl + rr * D_ + lane * 8), p1 = *(const f32x4*)(xl + rr * D_ + lane * 8 + 4);
      f32x4 p2 = *(const f32x4*)(xl + rr * D_ + c1), p3 = *(const f32x4*)(xl + rr * D_ + c1 + 4);
#pragma unroll
      for (int e = 0; e < 4; ++e) { v[e] = p0[e]; v[4 + e] = p1[e]; v[8 + e] = two ? p2[e] : 0.f; v[12 + e] = two ? p3[e] : 0.f; }
    }
    float sm = 0.f;
#pragma unroll
    for (int e = 0; e < 16; ++e) sm += v[e];
    sm = wave_sum(sm);
    const float mean = sm * (1.f / D_);
    float q = 0.f;
#pragma unroll
    for (int e = 0; e < 8; ++e) { float d = v[e] - mean; q += d * d; }
    if (two) {
#pragma unroll
      for (int e = 8; e < 16; ++e) { float d = v[e] - mean; q += d * d; }
    }
    q = wave_sum(q);
    const float mb = rbf(mean), vb = rbf(q * (1.f / D_));
    const float den = rbf(sqrtf(rbf(vb + 1e-5f)));
#pragma unroll
    for (int part = 0; part < 2; ++part) {
      if (part == 0 || two) {
        const int c0 = part * 512 + lane * 8;
        float o[8];
#pragma unroll
        for (int e = 0; e < 8; ++e) {
          float tv = rbf(rbf(v[part * 8 + e] - mb) / den);
          tv = rbf(tv * rbf(1.f + mcb[part][e >> 2][e & 3]));
          o[e] = tv + msb[part][e >> 2][e & 3];
        }
        const int off = ((s * 32 + t) * D_ + c0) * 2;
        v4u hv = {pack2(o[0], o[1]), pack2(o[2], o[3]), pack2(o[4], o[5]), pack2(o[6], o[7])};
        v4u xv = {pack2(v[part * 8 + 0], v[part * 8 + 1]), pack2(v[part * 8 + 2], v[part * 8 + 3]), pack2(v[part * 8 + 4], v[part * 8 + 5]), pack2(v[part * 8 + 6], v[part * 8 + 7])};
        __builtin_amdgcn_raw_buffer_store_b128(hv, rh2, off, 0, 16);
        __builtin_amdgcn_raw_buffer_store_b128(xv, rx1, off, 0, 16);
      }
    }
  }
}

__device__ __forceinline__ void task_f1(const MegaArgs& a, char* smem, int s, int nb) {
  constexpr int BM = 32, BN = 128;
  PGen P; P.A = (const u16*)(a.ws + WS_H2); P.lda = D_; P.W = a.w1; P.W2 = a.w3; P.K = D_; P.ldb = F_;
  const int m0 = s * BM, f0 = nb * 64;
  f32x4 acc[BM / 32][BN / 32];
  gemm_tile_dma<BM, BN, 10, 6, true>(P, m0, f0, smem, acc);
  const int tid = otid(); const int lane = tid & 63, wid = tid >> 6, wm = wid >> 1, wn = wid & 1, fr = lane & 15, g = lane >> 4;
  f32x4* ex = (f32x4*)smem;
  if (wn == 1) {
#pragma unroll
    for (int j = 0; j < BN / 32; ++j) ex[(wm * (BN / 32) + j) * 64 + lane] = acc[0][j];
  }
  __syncthreads();
  if (wn == 0) {
    const __amdgpu_buffer_rsrc_t rg = __builtin_amdgcn_make_buffer_rsrc(a.ws + WS_G, 0, S_ * F_ * 2, 0x00020000);
#pragma unroll
    for (int j = 0; j < BN / 32; j += 2) {
      v2u pk[2];
#pragma unroll
      for (int jj = 0; jj < 2; ++jj) {
        f32x4 up = ex[(wm * (BN / 32) + j + jj) * 64 + lane];
        f32x4 o;
#pragma unroll
        for (int r = 0; r < 4; ++r) {
          float gt = rbf(acc[0][j + jj][r]);
          float sg = rbf(gt / (1.f + __expf(-gt)));
          o[r] = sg * rbf(up[r]);
        }
        pk[jj] = pk4(o);
      }
      v4u v = pair16(pk[0], pk[1]);
      int tok = m0 + wm * (BM / 2) + fr, f = f0 + (j + (g & 1)) * 16 + (g >> 1) * 8;
      __builtin_amdgcn_raw_buffer_store_b128(v, rg, (tok * F_ + f) * 2, 0, 16);
    }
  }
  __syncthreads();
}

__device__ __forceinline__ void task_f2(const MegaArgs& a, char* smem, int s, int nb) {
  constexpr int BM = 32, BN = 64;
  PGen P; P.A = (const u16*)(a.ws + WS_G); P.lda = F_; P.W = a.w2; P.W2 = nullptr; P.K = F_; P.ldb = D_;
  const int m0 = s * BM, n0 = nb * BN;
  f32x4 acc[BM / 32][BN / 32];
  gemm_tile_dma<BM, BN, 24, 8, true>(P, m0, n0, smem, acc);
  const int tid = otid(); const int lane = tid & 63, wid = tid >> 6, wm = wid >> 1, wn = wid & 1, fr = lane & 15, g = lane >> 4;
  const u16* x1 = (const u16*)(a.ws + WS_X1);
  const float* gb = (const float*)(a.ws + WS_MOD) + 5 * D_;
#pragma unroll
  for (int j = 0; j < BN / 32; ++j) {
    int tok = m0 + wm * (BM / 2) + fr, c = n0 + wn * (BN / 2) + j * 16 + 4 * g;
    v2u xv = *(const v2u*)(x1 + (size_t)tok * D_ + c);
    float xo[4] = {bflo(xv[0]), bfhi(xv[0]), bflo(xv[1]), bfhi(xv[1])};
    f32x4 o;
#pragma unroll
    for (int r = 0; r < 4; ++r) o[r] = xo[r] + rbf(gb[c + r] * rbf(acc[0][j][r]));
    *(v2u*)(a.out + (size_t)tok * D_ + c) = pk4(o);
  }
}

constexpr int NP_S = 8 + 24 + 10;
constexpr int D1_ = 8, D2_ = 10;
constexpr int NT2_ = (NSEG_ + D2_) * NP_S;
__device__ __forceinline__ void worker_role(const MegaArgs& a, char* smem, const int x, const int j) {
  unsigned* tmo = ctl(a.ws, C_TMO);
  {
    const int nh = (x == 3) ? 4 : 7, h0 = (x == 3) ? 0 : 4 + (x - 4) * 7, nw = (x == 3) ? 16 : 32;
    const int bq0 = 112 + ((x == 3) ? 0 : 4 + (x - 4) * 11), nbx = (x == 3) ? 4 : 11;
    const int nsplit = 6 * nh + nbx;
    for (int e = j; e < nsplit + (NMT_ - 2) * nh; e += nw) {
      if (e >= 2 * nh && e < 2 * nh + nbx) {
        task_b(a, smem, bq0 + e - 2 * nh);
      } else if (e < nsplit) {
        const int e2 = e < 2 * nh ? e : e - nbx, row = e2 / nh, h = h0 + e2 % nh;
        const int mt = (row == 2 || row == 3 || row == 5) ? 1 : 0;
        if (row == 0 || row == 2) { task_qkvu<6, 3>(a, smem, mt, h); publish(ctl(a.ws, C_K + mt)); }
        else if (row == 1 || row == 3) { task_qkvu<3, 3>(a, smem, mt, h); publish(ctl(a.ws, C_K + mt)); }
        else { task_qkvu<0, 3>(a, smem, mt, h); publish(ctl(a.ws, C_Q0 + mt)); }
      } else {
        const int mt = 2 + (e - nsplit) / nh, h = h0 + (e - nsplit) % nh;
        TSTAMP(mt * 32 + h, 0); TSTAMP(mt * 32 + h, 1);
        task_qkvu<0, 9>(a, smem, mt, h);
        publish(ctl(a.ws, C_K + mt));
        TSTAMP(mt * 32 + h, 2);
      }
    }
  }
  volatile unsigned* lw = (volatile unsigned*)smem;
  for (;;) {
    __syncthreads();
    if (threadIdx.x == 0) lw[0] = __hip_atomic_fetch_add(ctl(a.ws, C_QUEUE), 1u, __ATOMIC_RELAXED, __HIP_MEMORY_SCOPE_AGENT);
    __syncthreads();
    const int t = __builtin_amdgcn_readfirstlane((int)lw[0]);
    __syncthreads();
    if (t >= NT2_) break;
    const int r = t % NP_S;
    const int s = t / NP_S - (r < 8 ? 0 : (r < 32 ? D1_ : D2_));
    if (s < 0 || s >= NSEG_) continue;
    TSTAMP(512 + s * NP_S + r, 0);
    if (r < 8) {
      const int hq = r;
      if (threadIdx.x == 0) { if (s < 8) spin_ge(ctl(a.ws, C_Q0 + (s >> 2)), 32u, tmo); spin_ge(ctl(a.ws, C_K + (s >> 2)), s < 8 ? 64u : 32u, tmo); spin_ge(ctl(a.ws, C_S + s), (unsigned)NCHAIN_, tmo); acquire_all(); }
      __syncthreads();
      TSTAMP(512 + s * NP_S + r, 1);
      task_x(a, smem, s, hq);
      publish(ctl(a.ws, C_T + s));
      if (threadIdx.x == 0) { spin_ge(ctl(a.ws, C_T + s), 8u, tmo); acquire_all(); }
      __syncthreads();
      TSTAMP(512 + s * NP_S + r, 3);
      task_xc(a, smem, s, hq);
      publish(ctl(a.ws, C_XS + s));
    } else if (r < 32) {
      if (threadIdx.x == 0) { spin_ge(ctl(a.ws, C_XS + s), 8u, tmo); acquire_all(); }
      __syncthreads();
      TSTAMP(512 + s * NP_S + r, 1);
      task_f1(a, smem, s, r - 8);
      publish(ctl(a.ws, C_FS + s));
    } else {
      if (threadIdx.x == 0) { spin_ge(ctl(a.ws, C_FS + s), 24u, tmo); acquire_all(); }
      __syncthreads();
      TSTAMP(512 + s * NP_S + r, 1);
      task_f2(a, smem, s, r - 32);
      __syncthreads();
    }
    TSTAMP(512 + s * NP_S + r, 2);
  }
}

__global__ void __launch_bounds__(256, 1) k_mega(MegaArgs a) {
  extern __shared__ __attribute__((aligned(16))) char smem[];
  const int x = blockIdx.x & 7, j = blockIdx.x >> 3;
  if (x < 3 || (x == 3 && j < 16)) {
    const int cidx = (x < 3) ? x * 32 + j : 96 + j;
    if (cidx < H_) score_role(a, smem, cidx);
    else u_role(a, smem, cidx);
  } else {
    worker_role(a, smem, x, x == 3 ? j - 16 : j);
  }
}

extern "C" void kernel_launch(void* const* d_in, const int* in_sizes, int n_in,
                              void* d_out, int out_size, void* d_ws, size_t ws_size,
                              hipStream_t stream) {
  const u16* x = (const u16*)d_in[0];
  const u16* mem0 = (const u16*)d_in[1];
  const u16* Wq = (const u16*)d_in[2];
  const u16* Wk = (const u16*)d_in[3];
  const u16* Wv = (const u16*)d_in[4];
  const u16* Wo = (const u16*)d_in[5];
  const u16* w1 = (const u16*)d_in[8];
  const u16* w3 = (const u16*)d_in[9];
  const u16* w2 = (const u16*)d_in[10];
  const u16* c = (const u16*)d_in[11];
  const u16* wmod = (const u16*)d_in[12];
  char* ws = (char*)d_ws;
  float* mod = (float*)(ws + WS_MOD);

  (void)hipFuncSetAttribute((const void*)k_mega, hipFuncAttributeMaxDynamicSharedMemorySize, MEGA_LDS);

  k_pre<<<240, 256, 0, stream>>>(c, wmod, mem0, ws);
  k_ln<<<S_ / 4, 256, 0, stream>>>(x, mod + 0 * D_, mod + 1 * D_, (u16*)(ws + WS_H1));
  MegaArgs ma; ma.x = x; ma.Wq = Wq; ma.Wk = Wk; ma.Wv = Wv; ma.Wo = Wo; ma.w1 = w1; ma.w3 = w3; ma.w2 = w2; ma.ws = ws; ma.out = (u16*)d_out;
  k_mega<<<NBLK_, 256, MEGA_LDS, stream>>>(ma);
}
```

```cpp
#include <hip/hip_runtime.h>
#include <hip/hip_bf16.h>
#include <stdint.h>

constexpr int S_ = 2048, D_ = 640, H_ = 32, DH_ = 192, NSEG_ = 64, F_ = 1536;
constexpr int HD_ = H_ * DH_;
constexpr int NU_ = 80;
constexpr int UROWS_ = S_ + D_;
constexpr int UCH_ = UROWS_ / 8;
constexpr int NCHAIN_ = H_ + NU_;
constexpr int NBLK_ = 256;
constexpr int NWRK_ = NBLK_ - NCHAIN_;
constexpr int NMT_ = 16;

typedef short bf16x8 __attribute__((ext_vector_type(8)));
typedef short bf16x4 __attribute__((ext_vector_type(4)));
typedef float f32x4 __attribute__((ext_vector_type(4)));
typedef unsigned v4u __attribute__((ext_vector_type(4)));
typedef unsigned v2u __attribute__((ext_vector_type(2)));
typedef unsigned short u16;
typedef short v4i16_t __attribute__((ext_vector_type(4)));

__device__ __forceinline__ float bf2f(u16 x) { return __uint_as_float(((unsigned)x) << 16); }
__device__ __forceinline__ float bflo(unsigned x) { return __uint_as_float(x << 16); }
__device__ __forceinline__ float bfhi(unsigned x) { return __uint_as_float(x & 0xFFFF0000u); }
__device__ __forceinline__ u16 f2bf(float f) { return __builtin_bit_cast(u16, (__bf16)f); }
__device__ __forceinline__ float rbf(float f) { return bf2f(f2bf(f)); }
typedef __bf16 bf2_t __attribute__((ext_vector_type(2)));
typedef float f2_t __attribute__((ext_vector_type(2)));
__device__ __forceinline__ unsigned pack2(float a, float b) { f2_t f = {a, b}; bf2_t r = __builtin_convertvector(f, bf2_t); return __builtin_bit_cast(unsigned, r); }
__device__ __forceinline__ unsigned canon2(unsigned x) { return x == 0xFFFFFFFFu ? 0xFFFFFFFEu : x; }
__device__ __forceinline__ unsigned umax4(unsigned m, v4u v) {
  m = max(max(m, v[0]), v[1]);
  return max(max(m, v[2]), v[3]);
}

constexpr size_t al256(size_t x) { return (x + 255) & ~(size_t)255; }
constexpr size_t WS_CTL  = 0;
constexpr size_t CTL_BYTES = 32768;
constexpr size_t WS_MEMB = CTL_BYTES;
constexpr size_t WS_PB   = WS_MEMB + (size_t)65 * 10240;
constexpr size_t WS_SENT_END = WS_PB + (size_t)64 * 20480;
constexpr size_t WS_MOD  = al256(WS_SENT_END);
constexpr size_t WS_H1   = al256(WS_MOD + 3840 * 4);
constexpr size_t WS_Q    = al256(WS_H1 + (size_t)S_ * D_ * 2);
constexpr size_t WS_K    = WS_Q + (size_t)S_ * HD_ * 2;
constexpr size_t WS_U    = WS_K + (size_t)S_ * HD_ * 2;
constexpr size_t WS_WT   = WS_U + (size_t)H_ * UCH_ * D_ * 8 * 2;
constexpr size_t WS_KM   = WS_WT + (size_t)2 * D_ * HD_ * 2;
constexpr size_t WS_UM   = WS_KM + (size_t)NSEG_ * 8 * HD_ * 2;
constexpr size_t WS_X1   = WS_UM + (size_t)NSEG_ * H_ * D_ * 8 * 2;
constexpr size_t WS_H2   = WS_X1 + (size_t)S_ * D_ * 2;
constexpr size_t WS_G    = WS_H2 + (size_t)S_ * D_ * 2;
constexpr size_t WS_XP   = WS_G + (size_t)S_ * F_ * 2;
constexpr size_t WS_END  = WS_XP + (size_t)NSEG_ * 8 * 32 * D_ * 4;
constexpr int C_TMO = 0, C_K = 1, C_S = 81, C_T = 145, C_QUEUE = 210, C_XS = 256, C_FS = 320, C_Q0 = 211, C_B = 213;
__device__ __forceinline__ unsigned* ctl(char* ws, int idx) { return (unsigned*)(ws + WS_CTL) + idx * 16; }

constexpr unsigned SPIN_CAP = 1u << 22;
#define SPIN_SLEEP 12

__device__ __forceinline__ void spin_ge(unsigned* c, unsigned target, unsigned* tmo) {
  unsigned spins = 0;
  while (__hip_atomic_load(c, __ATOMIC_RELAXED, __HIP_MEMORY_SCOPE_AGENT) < target) {
    __builtin_amdgcn_s_sleep(SPIN_SLEEP);
    if (++spins > SPIN_CAP) { __hip_atomic_store(tmo, 1u, __ATOMIC_RELAXED, __HIP_MEMORY_SCOPE_AGENT); break; }
  }
}
__device__ __forceinline__ void acquire_all() {
  __builtin_amdgcn_fence(__ATOMIC_ACQUIRE, "agent");
  asm volatile("s_waitcnt vmcnt(0)" ::: "memory");
}
__device__ __forceinline__ void publish(unsigned* c) {
  asm volatile("s_waitcnt vmcnt(0)" ::: "memory");
  __syncthreads();
  if (threadIdx.x == 64) __hip_atomic_fetch_add(c, 1u, __ATOMIC_RELAXED, __HIP_MEMORY_SCOPE_AGENT);
  __syncthreads();
}

__device__ __forceinline__ v4u pair16(v2u A, v2u B) {
  auto r0 = __builtin_amdgcn_permlane16_swap(A[0], B[0], false, false);
  auto r1 = __builtin_amdgcn_permlane16_swap(A[1], B[1], false, false);
  unsigned a0 = r0[0], b0 = r0[1], a1 = r1[0], b1 = r1[1];
  return (v4u){a0, a1, b0, b1};
}
__device__ __forceinline__ int otid() { int t = threadIdx.x; asm volatile("" : "+v"(t)); return t; }
__device__ __forceinline__ v2u pk4(f32x4 v) { return (v2u){pack2(v[0], v[1]), pack2(v[2], v[3])}; }

__global__ void __launch_bounds__(256) k_ln(const u16* __restrict__ x, const float* __restrict__ shift, const float* __restrict__ scale, u16* __restrict__ y) {
  const int lane = threadIdx.x & 63, w = threadIdx.x >> 6;
  const int row = blockIdx.x * 4 + w;
  const unsigned* xr = (const unsigned*)(x + (size_t)row * D_);
  float v[10];
  float s = 0.f;
#pragma unroll
  for (int i = 0; i < 5; ++i) { unsigned u = xr[lane + 64 * i]; v[2 * i] = bflo(u); v[2 * i + 1] = bfhi(u); s += v[2 * i] + v[2 * i + 1]; }
#pragma unroll
  for (int m = 32; m >= 1; m >>= 1) s += __shfl_xor(s, m);
  const float mean = s * (1.f / D_);
  float q = 0.f;
#pragma unroll
  for (int i = 0; i < 10; ++i) { float d = v[i] - mean; q += d * d; }
#pragma unroll
  for (int m = 32; m >= 1; m >>= 1) q += __shfl_xor(q, m);
  const float mb = rbf(mean), vb = rbf(q * (1.f / D_));
  const float den = rbf(sqrtf(rbf(vb + 1e-5f)));
  unsigned* yr = (unsigned*)(y + (size_t)row * D_);
#pragma unroll
  for (int i = 0; i < 5; ++i) {
    int c = 2 * (lane + 64 * i);
    float o[2];
#pragma unroll
    for (int e = 0; e < 2; ++e) {
      float t = rbf(rbf(v[2 * i + e] - mb) / den);
      t = rbf(t * rbf(1.f + scale[c + e]));
      o[e] = t + shift[c + e];
    }
    yr[lane + 64 * i] = pack2(o[0], o[1]);
  }
}

__device__ __forceinline__ int fswz(int r) { return (((r >> 1) & 1) << 1) | (((r >> 3) & 1) << 2); }

__device__ __forceinline__ bf16x8 tr_frag(const char* sub_base, int krow0, int c0, int lane) {
  const int g = lane >> 4, q = (lane & 15) >> 2, pp = lane & 3;
  v4i16_t lo, hi;
  { int r = krow0 + 8 * g + q; lo = __builtin_amdgcn_ds_read_tr16_b64_v4i16((__attribute__((address_space(3))) v4i16_t*)(sub_base + r * 128 + (((c0 + (pp >> 1)) ^ fswz(r)) * 16) + 8 * (pp & 1))); }
  { int r = krow0 + 8 * g + 4 + q; hi = __builtin_amdgcn_ds_read_tr16_b64_v4i16((__attribute__((address_space(3))) v4i16_t*)(sub_base + r * 128 + (((c0 + (pp >> 1)) ^ fswz(r)) * 16) + 8 * (pp & 1))); }
  return (bf16x8){lo[0], lo[1], lo[2], lo[3], hi[0], hi[1], hi[2], hi[3]};
}

__device__ __forceinline__ void glds16(const void* gsrc, unsigned lds_dst) {
  unsigned keep;
  asm volatile("s_mov_b32 %0, m0\n\ts_mov_b32 m0, %2\n\ts_nop 0\n\tglobal_load_lds_dwordx4 %1, off\n\ts_mov_b32 m0, %0"
               : "=&s"(keep) : "v"(gsrc), "s"(lds_dst) : "memory");
}
#define WAITV(n) asm volatile("s_waitcnt vmcnt(%0)" :: "n"(n) : "memory")
#define RAWBAR() do { asm volatile("s_waitcnt lgkmcnt(0)" ::: "memory"); __builtin_amdgcn_s_barrier(); asm volatile("" ::: "memory"); } while (0)
template <int BM, int BN, int NK, int NBUF, bool SWAP, class PT, class DEP>
__device__ __forceinline__ void gemm_tile_dma(const PT& P, int m0, int n0, char* smem, f32x4 (&acc)[BM / 32][BN / 32], DEP dep) {
  constexpr int ABYTES = BM * 128, BBYTES = BN * 128, STG = ABYTES + BBYTES;
  constexpr int NA = BM * 8 / 256, NB = BN * 8 / 256, NOP = NA + NB;
  const int tid = otid(), lane = tid & 63, wid = tid >> 6, wm = wid >> 1, wn = wid & 1;
  const u16* asrc[NA];
  const u16* bsrc[NB];
#pragma unroll
  for (int it = 0; it < NA; ++it) { int p = it * 256 + tid; int row = p >> 3, slot = p & 7; asrc[it] = P.a_row(m0 + row) + ((slot ^ (row & 7)) * 8); }
#pragma unroll
  for (int it = 0; it < NB; ++it) { int p = it * 256 + tid; int sub = p >> 9, r = (p >> 3) & 63, slot = p & 7; bsrc[it] = P.b_col(n0, sub) + (size_t)r * P.ldb + ((slot ^ fswz(r)) * 8); }
  const unsigned ldsw = __builtin_amdgcn_readfirstlane((unsigned)(size_t)smem + (tid >> 6) * 1024);
  auto stage_op = [&](int kt, int op) {
    const unsigned base = ldsw + (kt % NBUF) * STG;
    if (op < NA) glds16(asrc[op] + kt * 64, base + op * 4096);
    else glds16(bsrc[op - NA] + (size_t)kt * 64 * P.ldb, base + ABYTES + (op - NA) * 4096);
  };
#pragma unroll
  for (int i = 0; i < BM / 32; ++i)
#pragma unroll
    for (int j = 0; j < BN / 32; ++j) acc[i][j] = (f32x4){0.f, 0.f, 0.f, 0.f};
  const int fr = lane & 15, g = lane >> 4;
#pragma unroll
  for (int d = 0; d < NBUF - 1; ++d)
#pragma unroll
    for (int op = NA; op < NOP; ++op) stage_op(d, op);
  dep();
#pragma unroll
  for (int d = 0; d < NBUF - 1; ++d)
#pragma unroll
    for (int op = 0; op < NA; ++op) stage_op(d, op);
  constexpr int NI = BM / 32, NJ = BN / 32, NM = 2 * NI * NJ;
  constexpr int EVERY = NM / NOP;
#pragma unroll
  for (int kt = 0; kt < NK; ++kt) {
    const int after = (kt + NBUF - 2 < NK - 1 ? NBUF - 2 : NK - 1 - kt);
    if (after == 0 || kt == 0) WAITV(0); else if (after == 1) WAITV(NOP); else if (after == 2) WAITV(2 * NOP); else if (after == 3) WAITV(3 * NOP); else if (after == 4) WAITV(4 * NOP); else if (after == 5) WAITV(5 * NOP); else WAITV(6 * NOP);
    RAWBAR();
    char* base = smem + (kt % NBUF) * STG;
    char* bbase = base + ABYTES;
    bf16x8 af[2][NI], bf[2][NJ];
#pragma unroll
    for (int kk = 0; kk < 2; ++kk) {
#pragma unroll
      for (int j = 0; j < NJ; ++j) {
        int col = wn * (BN / 2) + j * 16;
        bf[kk][j] = tr_frag(bbase + (col >> 6) * 8192, kk * 32, (col & 63) >> 3, lane);
      }
#pragma unroll
      for (int i = 0; i < NI; ++i) {
        int row = wm * (BM / 2) + i * 16 + fr;
        af[kk][i] = *(const bf16x8*)(base + row * 128 + (((kk * 4 + g) ^ (row & 7)) * 16));
      }
    }
    __builtin_amdgcn_sched_barrier(0);
    if (kt + NBUF - 1 < NK) stage_op(kt + NBUF - 1, 0);
#pragma unroll
    for (int m = 0; m < NM; ++m) {
      const int kk = m / (NI * NJ), i = (m / NJ) % NI, j = m % NJ;
      acc[i][j] = SWAP ? __builtin_amdgcn_mfma_f32_16x16x32_bf16(bf[kk][j], af[kk][i], acc[i][j], 0, 0, 0)
                       : __builtin_amdgcn_mfma_f32_16x16x32_bf16(af[kk][i], bf[kk][j], acc[i][j], 0, 0, 0);
      if ((m + 1) % EVERY == 0 && (m + 1) / EVERY < NOP) {
        __builtin_amdgcn_sched_barrier(0);
        if (kt + NBUF - 1 < NK) stage_op(kt + NBUF - 1, (m + 1) / EVERY);
        __builtin_amdgcn_sched_barrier(0);
      }
    }
    __builtin_amdgcn_sched_barrier(0);
  }
  RAWBAR();
}

struct PGen {
  const u16* A; const u16* W; const u16* W2; int lda; int K; int ldb;
  __device__ const u16* a_row(int m) const { return A + (size_t)m * lda; }
  __device__ const u16* b_col(int n0, int sub) const { return W2 ? (sub ? W2 : W) + n0 : W + n0 + sub * 64; }
};

__global__ void __launch_bounds__(256) k_pre(const u16* __restrict__ c, const u16* __restrict__ wmod, const u16* __restrict__ mem0, char* ws) {
  __shared__ float red[16][17];
  __shared__ float sv[D_];
  const int b = blockIdx.x, tid = threadIdx.x;
  for (int k = tid; k < D_; k += 256) { float cv = bf2f(c[k]); sv[k] = rbf(cv / (1.f + __expf(-cv))); }
  if (b == 0) {
    u16* memb = (u16*)(ws + WS_MEMB);
    for (int gid = tid; gid < 640; gid += 256) { int u = gid >> 3, i = gid & 7; *(v4u*)(memb + gid * 8) = *(const v4u*)(mem0 + i * D_ + u * 8); }
  }
  {
    const v4u z = {0u, 0u, 0u, 0u}, f = {0xFFFFFFFFu, 0xFFFFFFFFu, 0xFFFFFFFFu, 0xFFFFFFFFu};
    for (size_t o = (size_t)(b * 256 + tid) * 16; o < CTL_BYTES; o += (size_t)240 * 256 * 16) *(v4u*)(ws + WS_CTL + o) = z;
    for (size_t o = (size_t)(b * 256 + tid) * 16; o < WS_SENT_END - WS_MEMB - 10240; o += (size_t)240 * 256 * 16) *(v4u*)(ws + WS_MEMB + 10240 + o) = f;
  }
  __syncthreads();
  const int col = tid & 15, kg = tid >> 4, n = b * 16 + col;
  float acc = 0.f;
#pragma unroll
  for (int k = 0; k < 40; ++k) acc += sv[kg * 40 + k] * bf2f(wmod[(size_t)(kg * 40 + k) * 3840 + n]);
  red[kg][col] = acc;
  __syncthreads();
  if (tid < 16) {
    float t = 0.f;
#pragma unroll
    for (int q = 0; q < 16; ++q) t += red[q][tid];
    ((float*)(ws + WS_MOD))[b * 16 + tid] = rbf(t);
  }
}

struct MegaArgs {
  const u16* x; const u16* Wq; const u16* Wk; const u16* Wv; const u16* Wo; const u16* w1; const u16* w3; const u16* w2;
  char* ws; u16* out;
};
constexpr int MEGA_LDS = 160 * 1024;
#define USLEEP 8
#define STAMP(id)
#define TSTAMP(t, id)
#define MFMA_AA(acc, wa, mb) asm volatile("v_mfma_f32_16x16x32_bf16 %0, %1, %2, %0" : "+v"(acc) : "a"(wa), "v"(mb))
#define MFMA_VV(acc, wa, mb) asm volatile("v_mfma_f32_16x16x32_bf16 %0, %1, %2, %0" : "+v"(acc) : "v"(wa), "v"(mb))
#define MFMA_BA(acc, ma, wb) asm volatile("v_mfma_f32_16x16x32_bf16 %0, %1, %2, %0" : "+v"(acc) : "v"(ma), "a"(wb))
#define MFMA_ACCA(acc, ma, wb) asm("v_mfma_f32_16x16x32_bf16 %0, %1, %2, %0" : "+a"(acc) : "v"(ma), "v"(wb))
#define MFMA_ACCV(acc, ma, wb) asm("v_mfma_f32_16x16x32_bf16 %0, %1, %2, %0" : "+v"(acc) : "v"(ma), "v"(wb))
#define MFMA16(acc, ma, wb) asm volatile("v_mfma_f32_16x16x16_bf16 %0, %1, %2, %0" : "+v"(acc) : "v"(ma), "v"(wb))

#define THROTTLE 0
#define PRE_U 3
#define PSLEEP 0
#define SSLEEP 0
#define PRE_P 3
#define PRE_NUM 3
__device__ __forceinline__ void presleep(unsigned long long t0, unsigned pre) {
  while ((unsigned)(__builtin_amdgcn_s_memtime() - t0) < pre) __builtin_amdgcn_s_sleep(2);
}
template <int NUM = PRE_NUM>
__device__ __forceinline__ unsigned nextpre(unsigned long long t0, unsigned& wmin) {
  unsigned wdur = (unsigned)(__builtin_amdgcn_s_memtime() - t0);
  wmin = min(wmin, wdur);
  return NUM * (wmin >> 3);
}
__device__ __forceinline__ v4u ror8(v4u v) {
  v4u r;
#pragma unroll
  for (int i = 0; i < 4; ++i) r[i] = (unsigned)__builtin_amdgcn_update_dpp(0, (int)v[i], 0x128, 0xf, 0xf, false);
  return r;
}
__device__ __forceinline__ float xmax8(float x) { return fmaxf(x, __builtin_bit_cast(float, __builtin_amdgcn_update_dpp(0, __builtin_bit_cast(int, x), 0x128, 0xf, 0xf, false))); }
__device__ __forceinline__ float xadd8(float x) { return x + __builtin_bit_cast(float, __builtin_amdgcn_update_dpp(0, __builtin_bit_cast(int, x), 0x128, 0xf, 0xf, false)); }
template <int N> __device__ __forceinline__ float dpp_ror_add(float x) { return x + __builtin_bit_cast(float, __builtin_amdgcn_update_dpp(0, __builtin_bit_cast(int, x), 0x120 + N, 0xf, 0xf, false)); }
__device__ __forceinline__ void swap16(float x, float& a, float& b) { auto r = __builtin_amdgcn_permlane16_swap(__float_as_uint(x), __float_as_uint(x), false, false); unsigned r0 = r[0], r1 = r[1]; a = __uint_as_float(r0); b = __uint_as_float(r1); }
__device__ __forceinline__ void swap32(float x, float& a, float& b) { auto r = __builtin_amdgcn_permlane32_swap(__float_as_uint(x), __float_as_uint(x), false, false); unsigned r0 = r[0], r1 = r[1]; a = __uint_as_float(r0); b = __uint_as_float(r1); }
__device__ __forceinline__ float xmax16(float x) { float a, b; swap16(x, a, b); return fmaxf(a, b); }
__device__ __forceinline__ float xadd16(float x) { float a, b; swap16(x, a, b); return a + b; }
__device__ __forceinline__ float xmax32(float x) { float a, b; swap32(x, a, b); return fmaxf(a, b); }
__device__ __forceinline__ float xadd32(float x) { float a, b; swap32(x, a, b); return a + b; }
__device__ __forceinline__ float wave_sum(float x) { x = dpp_ror_add<8>(x); x = dpp_ror_add<4>(x); x = dpp_ror_add<2>(x); x = dpp_ror_add<1>(x); return xadd32(xadd16(x)); }

__device__ __forceinline__ void wo_op_(const MegaArgs& a, char* smem, int tid, int h, int pass, int it) {
  const u16* wosrc = a.Wo + (size_t)h * DH_ * D_ + (size_t)(tid >> 3) * D_ + (((tid & 7) ^ fswz(tid >> 3)) * 8);
  const unsigned ldsw = __builtin_amdgcn_readfirstlane((unsigned)(size_t)smem + (tid >> 6) * 1024);
  glds16(wosrc + (size_t)it * 32 * D_ + pass * 64, ldsw + (pass & 3) * 24576 + it * 4096);
}
template <int NST0, int NST1>
__device__ __forceinline__ void u_gemm(const MegaArgs& a, char* smem, const int tid, const int urow0, const int h) {
  const int lane = tid & 63, wid = tid >> 6, wm = wid >> 1, wn = wid & 1, fr = lane & 15, g = lane >> 4;
  char* vimg = smem + 96 * 1024;
  const __amdgpu_buffer_rsrc_t ru = __builtin_amdgcn_make_buffer_rsrc(a.ws + WS_U, 0, H_ * UCH_ * D_ * 16, 0x00020000);
#pragma unroll
  for (int pass = 0; pass < 10; ++pass) {
    constexpr int N0[10] = {12, 16, 20, 24, 24, 24, 24, 24, 18, 12};
    if (pass < 3 && NST0 != NST1) { if (wn == 0) WAITV(N0[pass] + NST0); else WAITV(N0[pass] + NST1); } else if (pass < 3) WAITV(N0[pass] + NST0); else WAITV(N0[pass]);
    RAWBAR();
    char* bb = smem + (pass & 3) * 24576;
    f32x4 acc[4][2];
#pragma unroll
    for (int i = 0; i < 4; ++i) { acc[i][0] = (f32x4){0.f, 0.f, 0.f, 0.f}; acc[i][1] = (f32x4){0.f, 0.f, 0.f, 0.f}; }
    bf16x8 af[2][4], bf[2][2];
    auto rdfr = [&](int ks, int sl) {
      const int kt = ks >> 1, kk = ks & 1;
#pragma unroll
      for (int j = 0; j < 2; ++j) bf[sl][j] = tr_frag(bb + kt * 8192, kk * 32, (wn * 32 + j * 16) >> 3, lane);
#pragma unroll
      for (int i = 0; i < 4; ++i) { int row = wm * 64 + i * 16 + fr; af[sl][i] = *(const bf16x8*)(vimg + kt * 16384 + row * 128 + (((kk * 4 + g) ^ (row & 7)) * 16)); }
    };
    rdfr(0, 0);
#pragma unroll
    for (int ks = 0; ks < 6; ++ks) {
      if (ks + 1 < 6) rdfr(ks + 1, (ks + 1) & 1);
      if (pass + 3 < 10) wo_op_(a, smem, tid, h, pass + 3, ks);
      __builtin_amdgcn_sched_barrier(0);
#pragma unroll
      for (int i = 0; i < 4; ++i)
#pragma unroll
        for (int j = 0; j < 2; ++j) acc[i][j] = __builtin_amdgcn_mfma_f32_16x16x32_bf16(af[ks & 1][i], bf[ks & 1][j], acc[i][j], 0, 0, 0);
      __builtin_amdgcn_sched_barrier(0);
    }
#pragma unroll
    for (int i = 0; i < 4; i += 2)
#pragma unroll
      for (int j = 0; j < 2; ++j) {
        v4u v = pair16(pk4(acc[i][j]), pk4(acc[i + 1][j]));
        int row = urow0 + wm * 64 + (i + (g & 1)) * 16 + (g >> 1) * 8, c = pass * 64 + wn * 32 + j * 16 + fr;
        __builtin_amdgcn_raw_buffer_store_b128(v, ru, ((h * UCH_ + (row >> 3)) * D_ + c) * 16, 0, 16);
      }
  }
}

__device__ __forceinline__ void task_b(const MegaArgs& a, char* smem, const int q) {
  const int mb = q % 5, hh = q / 5;
  const int t_ = otid();
  const unsigned ldsw = __builtin_amdgcn_readfirstlane((unsigned)(size_t)smem + (t_ >> 6) * 1024);
#pragma unroll
  for (int it = 0; it < 12; ++it) {
    const int row = (it & 3) * 32 + (t_ >> 3);
    glds16(a.Wv + (size_t)(mb * 128 + row) * HD_ + hh * DH_ + (it >> 2) * 64 + (((t_ & 7) ^ (row & 7)) * 8), ldsw + 96 * 1024 + it * 4096);
  }
#pragma unroll
  for (int ps = 0; ps < 3; ++ps)
#pragma unroll
    for (int it = 0; it < 6; ++it) wo_op_(a, smem, t_, hh, ps, it);
  u_gemm<0, 0>(a, smem, t_, S_ + mb * 128, hh);
  publish(ctl(a.ws, C_B));
}

__device__ __forceinline__ void score_role(const MegaArgs& a, char* smem, const int b) {
  const int tid = threadIdx.x, lane = tid & 63, w = __builtin_amdgcn_readfirstlane(tid >> 6);
  const int fr = lane & 15, g = lane >> 4;
  const __amdgpu_buffer_rsrc_t rm = __builtin_amdgcn_make_buffer_rsrc(a.ws + WS_MEMB, 0, 65 * 10240, 0x00020000);
  const __amdgpu_buffer_rsrc_t rp = __builtin_amdgcn_make_buffer_rsrc(a.ws + WS_PB, 0, 64 * 20480, 0x00020000);
  const __amdgpu_buffer_rsrc_t rkm = __builtin_amdgcn_make_buffer_rsrc(a.ws + WS_KM, 0, NSEG_ * 8 * HD_ * 2, 0x00020000);
  const u16* Kx = (const u16*)(a.ws + WS_K);
  unsigned* tmo = ctl(a.ws, C_TMO);
  const int moff2 = (fr >> 3) * 512 + (g * 8 + (fr & 7)) * 16;
  const int h = b;
  char* lw = smem;
  float* part = (float*)(smem + 144 * 1024);
  char* lmem = smem + 149 * 1024;
  task_b(a, smem, NU_ + h);
  bf16x8 wa[64], wv[20];
  {
    const unsigned ldsw = __builtin_amdgcn_readfirstlane((unsigned)(size_t)smem + w * 1024);
    const size_t boff = (size_t)(tid >> 3) * HD_ + h * DH_ + (((tid & 7) ^ fswz(tid >> 3)) * 8);
    auto stage = [&](int ks) {
#pragma unroll
      for (int it = 0; it < 6; ++it) glds16((it < 3 ? a.Wq : a.Wk) + boff + (size_t)(ks * 32) * HD_ + (it % 3) * 64, ldsw + (ks & 3) * 24576 + it * 4096);
    };
    bf16x8* scr = (bf16x8*)(a.ws + WS_WT) + ((size_t)(h * 4 + w) * 36) * 64 + lane;
    stage(0); stage(1); stage(2);
#pragma unroll
    for (int ks = 0; ks < 20; ++ks) {
      if (ks < 18) WAITV(12); else if (ks == 18) WAITV(6); else WAITV(0);
      RAWBAR();
      if (ks + 3 < 20) {
        stage(ks + 3);
      }
#pragma unroll
      for (int p = 0; p < 6; ++p) {
        const int etg = 3 * w + (p % 3);
        bf16x8 v = tr_frag(smem + (ks & 3) * 24576 + (p / 3) * 12288 + (etg >> 2) * 4096, 0, (etg & 3) * 2, lane);
        const int f = p * 20 + ks;
        if (f < 64) wa[f] = v; else if (f < 84) wv[f - 64] = v; else scr[(f - 84) * 64] = v;
      }
    }
    asm volatile("s_waitcnt vmcnt(0)" ::: "memory");
    __syncthreads();
    for (int f = 0; f < 36; ++f) *(bf16x8*)(lw + (w * 36 + f) * 1024 + lane * 16) = scr[f * 64];
  }
  const char* lwp = lw + w * 36 * 1024 + lane * 16;
  const float scale = 0.07216878364870322f;
  unsigned pre_m = 0, wmin_m = 0xFFFFFFFFu;
  for (int s = 0; s < NSEG_; ++s) {
    if ((s & 3) == 0) {
      if (tid == 0) { spin_ge(ctl(a.ws, C_K + (s >> 2)), s < 8 ? 64u : 32u, tmo); acquire_all(); }
      __syncthreads();
    }
    STAMP(0);
    bf16x4 kx[2][3];
#pragma unroll
    for (int kt = 0; kt < 2; ++kt)
#pragma unroll
      for (int et = 0; et < 3; ++et)
        kx[kt][et] = *(const bf16x4*)(Kx + (size_t)(s * 32 + kt * 16 + fr) * HD_ + h * DH_ + (3 * w + et) * 16 + 4 * g);
    bf16x8 l5[2];
    l5[0] = *(const bf16x8*)(lwp + 16 * 1024);
    l5[1] = *(const bf16x8*)(lwp + 17 * 1024);
    {
      v4u mv[3];
      unsigned spins = 0;
      const unsigned long long t0 = __builtin_amdgcn_s_memtime();
      presleep(t0, pre_m);
      for (;;) {
        __builtin_amdgcn_s_sleep(SSLEEP);
        unsigned mx = 0;
#pragma unroll
        for (int k = 0; k < 3; ++k) { const int c = tid + 256 * k; mv[k] = __builtin_amdgcn_raw_buffer_load_b128(rm, s * 10240 + (c < 640 ? c : tid) * 16, 0, 16); }
#pragma unroll
        for (int k = 0; k < 3; ++k) mx = umax4(mx, mv[k]);
        if (!__any(mx == 0xFFFFFFFFu)) break;
        if (++spins > SPIN_CAP) { __hip_atomic_store(tmo, 1u, __ATOMIC_RELAXED, __HIP_MEMORY_SCOPE_AGENT); break; }
      }
      pre_m = nextpre(t0, wmin_m);
#pragma unroll
      for (int k = 0; k < 3; ++k) { const int c = tid + 256 * k; if (c < 640) *(v4u*)(lmem + c * 16) = mv[k]; }
    }
    __syncthreads();
    STAMP(1);
    f32x4 acc[6];
#pragma unroll
    for (int p = 0; p < 6; ++p) acc[p] = (f32x4){0.f, 0.f, 0.f, 0.f};
    bf16x8 l4[2], mf[3];
    l4[0] = *(const bf16x8*)(lwp + 0 * 1024);
    l4[1] = *(const bf16x8*)(lwp + 1 * 1024);
    const char* lmp = lmem + (g * 8 + (fr & 7)) * 16;
    mf[0] = *(const bf16x8*)(lmp); mf[1] = *(const bf16x8*)(lmp + 512); mf[2] = *(const bf16x8*)(lmp + 1024);
    asm volatile("s_nop 1");
#pragma unroll
    for (int ks = 0; ks < 20; ++ks) {
      const bf16x8 m = mf[ks % 3];
      MFMA_AA(acc[0], wa[ks], m);
      MFMA_AA(acc[1], wa[20 + ks], m);
      MFMA_AA(acc[2], wa[40 + ks], m);
      if (ks < 4) MFMA_AA(acc[3], wa[60 + ks], m); else MFMA_VV(acc[3], wv[ks - 4], m);
      if (ks < 4) MFMA_VV(acc[4], wv[16 + ks], m);
      else { MFMA_VV(acc[4], l4[ks & 1], m); if (ks + 2 < 20) l4[ks & 1] = *(const bf16x8*)(lwp + (ks + 2 - 4) * 1024); }
      MFMA_VV(acc[5], l5[ks & 1], m);
      if (ks + 2 < 20) l5[ks & 1] = *(const bf16x8*)(lwp + (16 + ks + 2) * 1024);
      if (ks + 3 < 20) mf[ks % 3] = *(const bf16x8*)(lmp + (ks + 3) * 512);
    }
    asm volatile("s_nop 7\n\ts_nop 1" : "+v"(acc[0]), "+v"(acc[1]), "+v"(acc[2]), "+v"(acc[3]), "+v"(acc[4]), "+v"(acc[5]));
    STAMP(3);
    bf16x4 qf[3], kf[3];
#pragma unroll
    for (int et = 0; et < 3; ++et) { qf[et] = __builtin_bit_cast(bf16x4, pk4(acc[et])); kf[et] = __builtin_bit_cast(bf16x4, pk4(acc[3 + et])); }
    f32x4 st[3];
#pragma unroll
    for (int kt = 0; kt < 3; ++kt) st[kt] = (f32x4){0.f, 0.f, 0.f, 0.f};
    asm volatile("s_nop 1" : "+v"(qf[0]), "+v"(qf[1]), "+v"(qf[2]), "+v"(kf[0]), "+v"(kf[1]), "+v"(kf[2]), "+v"(st[0]), "+v"(st[1]), "+v"(st[2]));
#pragma unroll
    for (int et = 0; et < 3; ++et) {
      MFMA16(st[0], kf[et], qf[et]);
      MFMA16(st[1], kx[0][et], qf[et]);
      MFMA16(st[2], kx[1][et], qf[et]);
    }
    asm volatile("s_nop 7\n\ts_nop 1" : "+v"(st[0]), "+v"(st[1]), "+v"(st[2]));
    float* pt = part;
    if (fr < 8) {
      if (g < 2) *(f32x4*)(pt + (w * 8 + fr) * 40 + 4 * g) = st[0];
      *(f32x4*)(pt + (w * 8 + fr) * 40 + 8 + 4 * g) = st[1];
      *(f32x4*)(pt + (w * 8 + fr) * 40 + 24 + 4 * g) = st[2];
    }
    STAMP(4);
    __syncthreads();
    STAMP(5);
    if (w == 0) {
      const int i = lane & 7, jc = lane >> 3;
      const int jr = jc < 5 ? jc : 0;
      float sc[8];
      f32x4 pp[8];
#pragma unroll
      for (int ww = 0; ww < 4; ++ww) { pp[2 * ww] = *(const f32x4*)(pt + (ww * 8 + i) * 40 + jr * 8); pp[2 * ww + 1] = *(const f32x4*)(pt + (ww * 8 + i) * 40 + jr * 8 + 4); }
      __builtin_amdgcn_sched_barrier(0);
#pragma unroll
      for (int e = 0; e < 4; ++e) { sc[e] = (pp[0][e] + pp[2][e]) + (pp[4][e] + pp[6][e]); sc[4 + e] = (pp[1][e] + pp[3][e]) + (pp[5][e] + pp[7][e]); }
      float mx = -3.0e38f;
#pragma unroll
      for (int e = 0; e < 8; ++e) { sc[e] = rbf(rbf(sc[e]) * scale); mx = fmaxf(mx, sc[e]); }
      mx = xmax8(mx); mx = xmax16(mx); mx = xmax32(mx);
      float sum = 0.f;
#pragma unroll
      for (int e = 0; e < 8; ++e) { sc[e] = __expf(sc[e] - mx); sum += sc[e]; }
      if (jc >= 5) sum = 0.f;
      sum = xadd8(sum); sum = xadd16(sum); sum = xadd32(sum);
      if (jc < 5) {
        const float inv = 1.f / sum;
        v4u o = {canon2(pack2(sc[0] * inv, sc[1] * inv)), canon2(pack2(sc[2] * inv, sc[3] * inv)), canon2(pack2(sc[4] * inv, sc[5] * inv)), canon2(pack2(sc[6] * inv, sc[7] * inv))};
        const int kc = (jc == 0) ? 128 + h : h * 4 + jc - 1;
        __builtin_amdgcn_raw_buffer_store_b128(o, rp, s * 20480 + kc * 128 + i * 16, 0, 16);
      }
    } else if (tid == 64 && s > 0) {
      __hip_atomic_fetch_add(ctl(a.ws, C_S + s - 1), 1u, __ATOMIC_RELAXED, __HIP_MEMORY_SCOPE_AGENT);
    }
    if (fr < 8) {
#pragma unroll
      for (int et = 0; et < 3; ++et)
        __builtin_amdgcn_raw_buffer_store_b64(__builtin_bit_cast(v2u, kf[et]), rkm, ((s * 8 + fr) * HD_ + h * DH_ + (3 * w + et) * 16 + 4 * g) * 2, 0, 16);
    }
    STAMP(6);
    __syncthreads();
  }
  publish(ctl(a.ws, C_S + NSEG_ - 1));
}

__device__ __forceinline__ void u_role(const MegaArgs& a, char* smem, const int b) {
  const int tid = threadIdx.x, lane = tid & 63, w = __builtin_amdgcn_readfirstlane(tid >> 6);
  const int fr = lane & 15, g = lane >> 4;
  const __amdgpu_buffer_rsrc_t rm = __builtin_amdgcn_make_buffer_rsrc(a.ws + WS_MEMB, 0, 65 * 10240, 0x00020000);
  const __amdgpu_buffer_rsrc_t rp = __builtin_amdgcn_make_buffer_rsrc(a.ws + WS_PB, 0, 64 * 20480, 0x00020000);
  const __amdgpu_buffer_rsrc_t rum = __builtin_amdgcn_make_buffer_rsrc(a.ws + WS_UM, 0, NSEG_ * H_ * D_ * 16, 0x00020000);
  const u16* U = (const u16*)(a.ws + WS_U);
  unsigned* tmo = ctl(a.ws, C_TMO);
  const int moff2 = (fr >> 3) * 512 + (g * 8 + (fr & 7)) * 16;
  const int u = b - H_;
  char* lub = smem;
  float* red = (float*)(smem + 4096);
  char* lmem = smem + 8192;
  task_b(a, smem, u);
  if (tid == 0) { spin_ge(ctl(a.ws, C_B), 160u, tmo); acquire_all(); }
  __syncthreads();
  bf16x8 ba[64], bv[16];
#pragma unroll
  for (int f = 0; f < 80; ++f) {
    const int nt = f / 20, ks = f % 20;
    const int hh = (4 * w + nt) * 2 + (fr >> 3), c = 8 * u + (fr & 7);
    bf16x8 v = *(const bf16x8*)(U + (((size_t)hh * UCH_ + 256 + ks * 4 + g) * D_ + c) * 8);
    if (f < 64) ba[f] = v; else bv[f - 64] = v;
  }
  unsigned pre_m = 0, pre_p = 0, wmin_m = 0xFFFFFFFFu, wmin_p = 0xFFFFFFFFu;
  for (int s = 0; s < NSEG_; ++s) {
    if ((s & 3) == 0) {
      if (tid == 0) { spin_ge(ctl(a.ws, C_K + (s >> 2)), s < 8 ? 64u : 32u, tmo); acquire_all(); }
      __syncthreads();
    }
    STAMP(0);
    bf16x8 ub[10];
#pragma unroll
    for (int k = 0; k < 8; ++k) ub[k] = *(const bf16x8*)(U + (((size_t)(8 * w + k) * UCH_ + s * 4 + g) * D_ + 8 * u + (fr & 7)) * 8);
    {
      v4u mv[3];
      unsigned spins = 0;
      const unsigned long long t0 = __builtin_amdgcn_s_memtime();
      presleep(t0, pre_m);
      for (;;) {
        __builtin_amdgcn_s_sleep(USLEEP);
        unsigned mx = 0;
#pragma unroll
        for (int k = 0; k < 3; ++k) { const int c = tid + 256 * k; mv[k] = __builtin_amdgcn_raw_buffer_load_b128(rm, s * 10240 + (c < 640 ? c : tid) * 16, 0, 16); }
#pragma unroll
        for (int k = 0; k < 3; ++k) mx = umax4(mx, mv[k]);
        if (!__any(mx == 0xFFFFFFFFu)) break;
        if (++spins > SPIN_CAP) { __hip_atomic_store(tmo, 1u, __ATOMIC_RELAXED, __HIP_MEMORY_SCOPE_AGENT); break; }
      }
      pre_m = nextpre<PRE_U>(t0, wmin_m);
#pragma unroll
      for (int k = 0; k < 3; ++k) { const int c = tid + 256 * k; if (c < 640) *(v4u*)(lmem + c * 16) = mv[k]; }
    }
    __syncthreads();
    STAMP(1);
    {
      f32x4 acc[4];
#pragma unroll
      for (int nt = 0; nt < 4; ++nt) acc[nt] = (f32x4){0.f, 0.f, 0.f, 0.f};
      const char* lmp = lmem + (g * 8 + (fr & 7)) * 16;
      bf16x8 mf[3];
      mf[0] = *(const bf16x8*)(lmp); mf[1] = *(const bf16x8*)(lmp + 512); mf[2] = *(const bf16x8*)(lmp + 1024);
      asm volatile("s_nop 1");
#pragma unroll
      for (int ks = 0; ks < 20; ++ks) {
        const bf16x8 m = mf[ks % 3];
        MFMA_BA(acc[0], m, ba[ks]);
        MFMA_BA(acc[1], m, ba[20 + ks]);
        MFMA_BA(acc[2], m, ba[40 + ks]);
        if (ks < 4) MFMA_BA(acc[3], m, ba[60 + ks]); else MFMA_VV(acc[3], m, bv[ks - 4]);
        if (ks + 3 < 20) mf[ks % 3] = *(const bf16x8*)(lmp + (ks + 3) * 512);
      }
      asm volatile("s_nop 7\n\ts_nop 1" : "+v"(acc[0]), "+v"(acc[1]), "+v"(acc[2]), "+v"(acc[3]));
      if (g < 2) {
#pragma unroll
        for (int nt = 0; nt < 4; ++nt) {
          int hh = (4 * w + nt) * 2 + (fr >> 3), cc = fr & 7;
          v2u v = pk4(acc[nt]);
          *(v2u*)(lub + (hh * 8 + cc) * 16 + g * 8) = v;
          __builtin_amdgcn_raw_buffer_store_b64(v, rum, (((s * H_ + hh) * D_ + 8 * u + cc) * 8 + g * 4) * 2, 0, 16);
        }
      }
    }
    STAMP(2);
    __syncthreads();
    if (tid == 64 && s > 0) __hip_atomic_fetch_add(ctl(a.ws, C_S + s - 1), 1u, __ATOMIC_RELAXED, __HIP_MEMORY_SCOPE_AGENT);
#pragma unroll
    for (int k = 0; k < 2; ++k) ub[8 + k] = *(const bf16x8*)(lub + ((4 * (2 * w + k) + g) * 8 + (fr & 7)) * 16);
    STAMP(3);
    v4u pv[10];
    {
      unsigned spins = 0;
      const unsigned long long t0 = __builtin_amdgcn_s_memtime();
      presleep(t0, pre_p);
      for (;;) {
        __builtin_amdgcn_s_sleep(PSLEEP);
        unsigned mx = 0;
#pragma unroll
        for (int k = 0; k < 4; ++k) pv[2 * k] = __builtin_amdgcn_raw_buffer_load_b128(rp, s * 20480 + (8 * w + 2 * k) * 512 + moff2, 0, 16);
        pv[8] = __builtin_amdgcn_raw_buffer_load_b128(rp, s * 20480 + (32 + 2 * w) * 512 + moff2, 0, 16);
#pragma unroll
        for (int k = 0; k < 5; ++k) mx = umax4(mx, pv[2 * k]);
        if (!__any(mx == 0xFFFFFFFFu)) break;
        if (++spins > SPIN_CAP) { __hip_atomic_store(tmo, 1u, __ATOMIC_RELAXED, __HIP_MEMORY_SCOPE_AGENT); break; }
      }
      pre_p = nextpre<PRE_P>(t0, wmin_p);
#pragma unroll
      for (int k = 0; k < 5; ++k) pv[2 * k + 1] = ror8(pv[2 * k]);
    }
    STAMP(4);
    f32x4 acc0 = {0.f, 0.f, 0.f, 0.f}, acc1 = {0.f, 0.f, 0.f, 0.f};
    asm volatile("s_nop 1" : "+v"(acc0), "+v"(acc1), "+v"(pv[1]), "+v"(pv[3]), "+v"(pv[5]), "+v"(pv[7]), "+v"(pv[9]));
#pragma unroll
    for (int k = 0; k < 10; k += 2) {
      MFMA_VV(acc0, ub[k], __builtin_bit_cast(bf16x8, pv[k]));
      MFMA_VV(acc1, ub[k + 1], __builtin_bit_cast(bf16x8, pv[k + 1]));
    }
    asm volatile("s_nop 7\n\ts_nop 1" : "+v"(acc0), "+v"(acc1));
    acc0 += acc1;
    if (fr < 8 && g < 2) *(f32x4*)(red + (w * 8 + fr) * 8 + 4 * g) = acc0;
    __syncthreads();
    STAMP(5);
    if (tid < 16) {
      const int i = tid >> 1, hf = tid & 1;
      f32x4 o = *(const f32x4*)(red + (0 * 8 + i) * 8 + 4 * hf);
#pragma unroll
      for (int ww = 1; ww < 4; ++ww) o += *(const f32x4*)(red + (ww * 8 + i) * 8 + 4 * hf);
      v2u v = {canon2(pack2(o[0], o[1])), canon2(pack2(o[2], o[3]))};
      __builtin_amdgcn_raw_buffer_store_b64(v, rm, (s + 1) * 10240 + u * 128 + tid * 8, 0, 16);
    }
    STAMP(6);
  }
  publish(ctl(a.ws, C_S + NSEG_ - 1));
}

#define QKV_COMPUTE(BASE, KN) do { \
    char* base_ = (BASE); \
    bf16x8 af[4], bf[4]; \
    _Pragma("unroll") for (int i = 0; i < 4; ++i) { int row = wm * 64 + i * 16 + fr; af[i] = *(const bf16x8*)(base_ + row * 64 + ((g ^ (((row >> 3) & 1) * 3)) * 16)); } \
    _Pragma("unroll") for (int j = 0; j < 4; ++j) { const int nt = wn * NTW + j; bf[j] = tr_frag(base_ + 8192 + (nt >> 2) * 4096, 0, (nt & 3) * 2, lane); } \
    _Pragma("unroll") for (int j = 0; j < NTW; ++j) { \
      __builtin_amdgcn_sched_barrier(0); \
      _Pragma("unroll") for (int i = 0; i < 4; ++i) { if (i < 3 || j < 10) MFMA_ACCA(acc[i][j], bf[j & 3], af[i]); else MFMA_ACCV(acc[i][j], bf[j & 3], af[i]); } \
      __builtin_amdgcn_sched_barrier(0); \
      if (j + 4 < NTW) { const int nt = wn * NTW + j + 4; bf[j & 3] = tr_frag(base_ + 8192 + (nt >> 2) * 4096, 0, (nt & 3) * 2, lane); } \
      if ((KN) >= 0) { const int o0 = (j * NOPS + NTW - 1) / NTW, o1 = ((j + 1) * NOPS + NTW - 1) / NTW; if (o1 > o0 && o0 < NOPS) stage_op((KN), o0); } \
    } } while (0)
template <int SUB0, int NS>
__device__ __forceinline__ void task_qkvu(const MegaArgs& a, char* smem, int mt, int h) {
  constexpr int BM = 128, NTW = 2 * NS, NOPS = 2 + NS;
  constexpr bool HASV = (SUB0 + NS == 9);
  const int tid = otid(), lane = tid & 63, wid = tid >> 6, wm = wid >> 1, wn = wid & 1, fr = lane & 15, g = lane >> 4;
  char* vimg = smem + 96 * 1024;
  const unsigned ldsw = __builtin_amdgcn_readfirstlane((unsigned)(size_t)smem + (tid >> 6) * 1024);
  auto wo_stage = [&](int pass) {
#pragma unroll
    for (int it = 0; it < 6; ++it) wo_op_(a, smem, tid, h, pass, it);
  };
  {
    constexpr int STG = 8192 + NS * 4096;
    const u16* asrc = (const u16*)(a.ws + WS_H1) + (size_t)(mt * BM + (tid >> 2)) * D_ + (((tid & 3) ^ (((tid >> 5) & 1) * 3)) * 8);
    const size_t boff = (size_t)(tid >> 3) * HD_ + h * DH_ + (((tid & 7) ^ fswz(tid >> 3)) * 8);
    auto stage_op = [&](int kt, int op) {
      const unsigned base = ldsw + (kt % 3) * STG;
      if (op < 2) glds16(asrc + (size_t)op * 64 * D_ + kt * 32, base + op * 4096);
      else {
        const int it = op - 2, sb = SUB0 + it;
        const u16* W = (sb < 3) ? a.Wq : (sb < 6 ? a.Wk : a.Wv);
        glds16(W + boff + (size_t)(kt * 32) * HD_ + (sb % 3) * 64, base + 8192 + it * 4096);
      }
    };
    f32x4 acc[4][NTW];
#pragma unroll
    for (int i = 0; i < 4; ++i)
#pragma unroll
      for (int j = 0; j < NTW; ++j) acc[i][j] = (f32x4){0.f, 0.f, 0.f, 0.f};
#pragma unroll
    for (int op = 0; op < NOPS; ++op) stage_op(0, op);
#pragma unroll
    for (int op = 0; op < NOPS; ++op) stage_op(1, op);
    for (int kb = 0; kb < 18; kb += 3) {
#pragma unroll
      for (int d = 0; d < 3; ++d) {
        const int kt = kb + d;
        WAITV(NOPS);
        RAWBAR();
        QKV_COMPUTE(smem + d * STG, kt + 2);
      }
    }
    WAITV(NOPS); RAWBAR(); QKV_COMPUTE(smem + 0 * STG, -1);
    WAITV(0); RAWBAR(); QKV_COMPUTE(smem + 1 * STG, -1);
    RAWBAR();
#pragma unroll
    for (int j = 0; j < NTW; ++j) asm volatile("s_nop 7\n\ts_nop 1" : "+a"(acc[0][j]), "+a"(acc[1][j]), "+a"(acc[2][j]));
#pragma unroll
    for (int j = 0; j < NTW; ++j) { if (j < 10) asm volatile("s_nop 7\n\ts_nop 1" : "+a"(acc[3][j])); else asm volatile("s_nop 7\n\ts_nop 1" : "+v"(acc[3][j])); }
    TSTAMP(mt * 32 + h, 3);
    if (HASV) { wo_stage(0); wo_stage(1); wo_stage(2); }
    const __amdgpu_buffer_rsrc_t rq = __builtin_amdgcn_make_buffer_rsrc(a.ws + WS_Q, 0, 2 * S_ * HD_ * 2, 0x00020000);
#pragma unroll
    for (int j = 0; j < NTW; j += 2) {
      const int nt = SUB0 * 4 + wn * NTW + j;
      const int mat = nt / 12, nl = nt - mat * 12;
#pragma unroll
      for (int i = 0; i < 4; ++i) {
        if (mat < 2) {
          v4u v = pair16(pk4(acc[i][j]), pk4(acc[i][j + 1]));
          int tok = mt * BM + wm * 64 + i * 16 + fr, n = h * DH_ + (nl + (g & 1)) * 16 + (g >> 1) * 8;
          __builtin_amdgcn_raw_buffer_store_b128(v, rq, mat * (S_ * HD_ * 2) + (tok * HD_ + n) * 2, 0, 16);
        } else if (HASV) {
#pragma unroll
          for (int jj = 0; jj < 2; ++jj) {
            int row = wm * 64 + i * 16 + fr, n = (nl + jj) * 16 + 4 * g;
            *(v2u*)(vimg + (n >> 6) * 16384 + row * 128 + ((((n >> 3) & 7) ^ (row & 7)) * 16) + ((n >> 2) & 1) * 8) = pk4(acc[i][j + jj]);
          }
        }
      }
    }
  }
  if (HASV) u_gemm<(NS == 9 ? 36 : 0), (NS == 9 ? 12 : 0)>(a, smem, tid, mt * BM, h);
}

__device__ __forceinline__ void task_x(const MegaArgs& a, char* smem, int s, int hq) {
  const int tid = otid(), lane = tid & 63, w = tid >> 6, fr = lane & 15, g = lane >> 4;
  const u16* Q = (const u16*)(a.ws + WS_Q);
  const u16* Kx = (const u16*)(a.ws + WS_K);
  const u16* km = (const u16*)(a.ws + WS_KM);
  const u16* U = (const u16*)(a.ws + WS_U);
  const u16* Um = (const u16*)(a.ws + WS_UM);
  constexpr int PSTR = 336;
  const float scale = 0.07216878364870322f;
  bf16x8 uf[5][10];
  bf16x8 kfa[6][3], qfa[6][2];
#pragma unroll
  for (int ks = 0; ks < 6; ++ks) {
    const int eo = (4 * hq + w) * DH_ + ks * 32 + 8 * g;
    kfa[ks][1] = *(const bf16x8*)(Kx + (size_t)(s * 32 + fr) * HD_ + eo);
    kfa[ks][2] = *(const bf16x8*)(Kx + (size_t)(s * 32 + 16 + fr) * HD_ + eo);
    qfa[ks][0] = *(const bf16x8*)(Q + (size_t)(s * 32 + fr) * HD_ + eo);
    qfa[ks][1] = *(const bf16x8*)(Q + (size_t)(s * 32 + 16 + fr) * HD_ + eo);
  }
#pragma unroll
  for (int ks = 0; ks < 5; ++ks) {
    const int kc = ks * 4 + g, hl = kc / 5, jc = kc - hl * 5, h = 4 * hq + hl;
    const u16* ub = U + (((size_t)h * UCH_ + s * 4 + (jc == 0 ? 0 : jc - 1)) * D_) * 8 + (size_t)(160 * w + fr) * 8;
#pragma unroll
    for (int ct = 0; ct < 10; ++ct) uf[ks][ct] = *(const bf16x8*)(ub + ct * 128);
  }
  if (threadIdx.x == 0) { spin_ge(ctl(a.ws, C_S + s), (unsigned)NCHAIN_, ctl(a.ws, C_TMO)); acquire_all(); }
  __syncthreads();
#pragma unroll
  for (int ks = 0; ks < 6; ++ks) kfa[ks][0] = *(const bf16x8*)(km + ((size_t)s * 8 + (fr & 7)) * HD_ + (4 * hq + w) * DH_ + ks * 32 + 8 * g);
#pragma unroll
  for (int ks = 0; ks < 4; ++ks) {
    if (g == ks) {
      const u16* ub = Um + (((size_t)s * H_ + 4 * hq + ks) * D_) * 8 + (size_t)(160 * w + fr) * 8;
#pragma unroll
      for (int ct = 0; ct < 10; ++ct) uf[ks][ct] = *(const bf16x8*)(ub + ct * 128);
    }
  }
  __builtin_amdgcn_sched_barrier(0);
  {
    f32x4 acc[3][2];
#pragma unroll
    for (int kt = 0; kt < 3; ++kt) { acc[kt][0] = (f32x4){0.f, 0.f, 0.f, 0.f}; acc[kt][1] = (f32x4){0.f, 0.f, 0.f, 0.f}; }
#pragma unroll
    for (int ks = 0; ks < 6; ++ks)
#pragma unroll
      for (int kt = 0; kt < 3; ++kt)
#pragma unroll
        for (int tt = 0; tt < 2; ++tt) acc[kt][tt] = __builtin_amdgcn_mfma_f32_16x16x32_bf16(kfa[ks][kt], qfa[ks][tt], acc[kt][tt], 0, 0, 0);
#pragma unroll
    for (int tt = 0; tt < 2; ++tt) {
      const int t = tt * 16 + fr;
      float sc[12]; bool ok[12];
      float mx = -3.0e38f;
#pragma unroll
      for (int kt = 0; kt < 3; ++kt)
#pragma unroll
        for (int r = 0; r < 4; ++r) {
          int jl = 4 * g + r;
          bool v = (kt == 0) ? (jl < 8) : ((kt - 1) * 16 + jl <= t);
          float xx = rbf(rbf(acc[kt][tt][r]) * scale);
          sc[kt * 4 + r] = xx; ok[kt * 4 + r] = v;
          if (v) mx = fmaxf(mx, xx);
        }
      mx = xmax16(mx); mx = xmax32(mx);
      float sum = 0.f;
#pragma unroll
      for (int e = 0; e < 12; ++e) { sc[e] = ok[e] ? __expf(sc[e] - mx) : 0.f; sum += sc[e]; }
      sum = xadd16(sum); sum = xadd32(sum);
      const float inv = 1.f / sum;
      char* dst = smem + t * PSTR + w * 80;
      if (g < 2) *(v2u*)(dst + 8 * g) = (v2u){pack2(sc[0] * inv, sc[1] * inv), pack2(sc[2] * inv, sc[3] * inv)};
      *(v2u*)(dst + 16 + 8 * g) = (v2u){pack2(sc[4] * inv, sc[5] * inv), pack2(sc[6] * inv, sc[7] * inv)};
      *(v2u*)(dst + 48 + 8 * g) = (v2u){pack2(sc[8] * inv, sc[9] * inv), pack2(sc[10] * inv, sc[11] * inv)};
    }
  }
  __syncthreads();
  f32x4 acc[10][2];
#pragma unroll
  for (int ct = 0; ct < 10; ++ct) { acc[ct][0] = (f32x4){0.f, 0.f, 0.f, 0.f}; acc[ct][1] = (f32x4){0.f, 0.f, 0.f, 0.f}; }
#pragma unroll
  for (int ks = 0; ks < 5; ++ks) {
    bf16x8 pf[2];
    pf[0] = *(const bf16x8*)(smem + fr * PSTR + (ks * 4 + g) * 16);
    pf[1] = *(const bf16x8*)(smem + (16 + fr) * PSTR + (ks * 4 + g) * 16);
#pragma unroll
    for (int ct = 0; ct < 10; ++ct)
#pragma unroll
      for (int tt = 0; tt < 2; ++tt) acc[ct][tt] = __builtin_amdgcn_mfma_f32_16x16x32_bf16(uf[ks][ct], pf[tt], acc[ct][tt], 0, 0, 0);
  }
  const __amdgpu_buffer_rsrc_t rx = __builtin_amdgcn_make_buffer_rsrc(a.ws + WS_XP, 0, NSEG_ * 8 * 32 * D_ * 4, 0x00020000);
#pragma unroll
  for (int ct = 0; ct < 10; ++ct)
#pragma unroll
    for (int tt = 0; tt < 2; ++tt) {
      const int t = tt * 16 + fr, c = 160 * w + ct * 16 + 4 * g;
      __builtin_amdgcn_raw_buffer_store_b128(__builtin_bit_cast(v4u, acc[ct][tt]), rx, (((s * 8 + hq) * 32 + t) * D_ + c) * 4, 0, 16);
    }
}

__device__ __forceinline__ void task_xc(const MegaArgs& a, char* smem, int s, int q8) {
  const int tid = otid(), lane = tid & 63, w = tid >> 6;
  const float* mod = (const float*)(a.ws + WS_MOD);
  const float* xp = (const float*)(a.ws + WS_XP) + (size_t)s * 8 * 32 * D_;
  const __amdgpu_buffer_rsrc_t rx1 = __builtin_amdgcn_make_buffer_rsrc(a.ws + WS_X1, 0, S_ * D_ * 2, 0x00020000);
  const __amdgpu_buffer_rsrc_t rh2 = __builtin_amdgcn_make_buffer_rsrc(a.ws + WS_H2, 0, S_ * D_ * 2, 0x00020000);
  float* xl = (float*)smem + w * D_;
  const int t0 = 4 * q8 + w;
  const bool two = lane < 16;
  const int c1 = two ? 512 + lane * 8 : lane * 8;
  f32x4 msb[2][2], mcb[2][2];
  {
    f32x4 pp[2][8][2]; v4u xx[2]; f32x4 gg[2][2];
#pragma unroll
    for (int k = 0; k < 2; ++k) {
      const int c0 = k ? c1 : lane * 8;
#pragma unroll
      for (int pq = 0; pq < 8; ++pq) { pp[k][pq][0] = *(const f32x4*)(xp + ((size_t)pq * 32 + t0) * D_ + c0); pp[k][pq][1] = *(const f32x4*)(xp + ((size_t)pq * 32 + t0) * D_ + c0 + 4); }
      xx[k] = *(const v4u*)(a.x + (size_t)(s * 32 + t0) * D_ + c0);
      gg[k][0] = *(const f32x4*)(mod + 2 * D_ + c0); gg[k][1] = *(const f32x4*)(mod + 2 * D_ + c0 + 4);
      msb[k][0] = *(const f32x4*)(mod + 3 * D_ + c0); msb[k][1] = *(const f32x4*)(mod + 3 * D_ + c0 + 4);
      mcb[k][0] = *(const f32x4*)(mod + 4 * D_ + c0); mcb[k][1] = *(const f32x4*)(mod + 4 * D_ + c0 + 4);
    }
    __builtin_amdgcn_sched_barrier(0);
#pragma unroll
    for (int k = 0; k < 2; ++k) {
      f32x4 s0 = ((pp[k][0][0] + pp[k][1][0]) + (pp[k][2][0] + pp[k][3][0])) + ((pp[k][4][0] + pp[k][5][0]) + (pp[k][6][0] + pp[k][7][0]));
      f32x4 s1 = ((pp[k][0][1] + pp[k][1][1]) + (pp[k][2][1] + pp[k][3][1])) + ((pp[k][4][1] + pp[k][5][1]) + (pp[k][6][1] + pp[k][7][1]));
      f32x4 o0, o1;
#pragma unroll
      for (int e = 0; e < 4; ++e) {
        float x0 = (e & 1) ? bfhi(xx[k][e >> 1]) : bflo(xx[k][e >> 1]);
        float x1v = (e & 1) ? bfhi(xx[k][2 + (e >> 1)]) : bflo(xx[k][2 + (e >> 1)]);
        o0[e] = rbf(x0 + rbf(gg[k][0][e] * rbf(s0[e])));
        o1[e] = rbf(x1v + rbf(gg[k][1][e] * rbf(s1[e])));
      }
      if (k == 0 || two) { const int c0 = k ? c1 : lane * 8; *(f32x4*)(xl + c0) = o0; *(f32x4*)(xl + c0 + 4) = o1; }
    }
  }
#pragma unroll
  for (int rr = 0; rr < 1; ++rr) {
    const int t = t0 + rr;
    float v[16];
    {
      f32x4 p0 = *(const f32x4*)(xl + rr * D_ + lane * 8), p1 = *(const f32x4*)(xl + rr * D_ + lane * 8 + 4);
      f32x4 p2 = *(const f32x4*)(xl + rr * D_ + c1), p3 = *(const f32x4*)(xl + rr * D_ + c1 + 4);
#pragma unroll
      for (int e = 0; e < 4; ++e) { v[e] = p0[e]; v[4 + e] = p1[e]; v[8 + e] = two ? p2[e] : 0.f; v[12 + e] = two ? p3[e] : 0.f; }
    }
    float sm = 0.f;
#pragma unroll
    for (int e = 0; e < 16; ++e) sm += v[e];
    sm = wave_sum(sm);
    const float mean = sm * (1.f / D_);
    float q = 0.f;
#pragma unroll
    for (int e = 0; e < 8; ++e) { float d = v[e] - mean; q += d * d; }
    if (two) {
#pragma unroll
      for (int e = 8; e < 16; ++e) { float d = v[e] - mean; q += d * d; }
    }
    q = wave_sum(q);
    const float mb = rbf(mean), vb = rbf(q * (1.f / D_));
    const float den = rbf(sqrtf(rbf(vb + 1e-5f)));
#pragma unroll
    for (int part = 0; part < 2; ++part) {
      if (part == 0 || two) {
        const int c0 = part * 512 + lane * 8;
        float o[8];
#pragma unroll
        for (int e = 0; e < 8; ++e) {
          float tv = rbf(rbf(v[part * 8 + e] - mb) / den);
          tv = rbf(tv * rbf(1.f + mcb[part][e >> 2][e & 3]));
          o[e] = tv + msb[part][e >> 2][e & 3];
        }
        const int off = ((s * 32 + t) * D_ + c0) * 2;
        v4u hv = {pack2(o[0], o[1]), pack2(o[2], o[3]), pack2(o[4], o[5]), pack2(o[6], o[7])};
        v4u xv = {pack2(v[part * 8 + 0], v[part * 8 + 1]), pack2(v[part * 8 + 2], v[part * 8 + 3]), pack2(v[part * 8 + 4], v[part * 8 + 5]), pack2(v[part * 8 + 6], v[part * 8 + 7])};
        __builtin_amdgcn_raw_buffer_store_b128(hv, rh2, off, 0, 16);
        __builtin_amdgcn_raw_buffer_store_b128(xv, rx1, off, 0, 16);
      }
    }
  }
}

__device__ __forceinline__ void task_f1(const MegaArgs& a, char* smem, int s, int nb) {
  constexpr int BM = 32, BN = 128;
  PGen P; P.A = (const u16*)(a.ws + WS_H2); P.lda = D_; P.W = a.w1; P.W2 = a.w3; P.K = D_; P.ldb = F_;
  const int m0 = s * BM, f0 = nb * 64;
  f32x4 acc[BM / 32][BN / 32];
  gemm_tile_dma<BM, BN, 10, 6, true>(P, m0, f0, smem, acc, [&]() {
    if (threadIdx.x == 0) { spin_ge(ctl(a.ws, C_XS + s), 8u, ctl(a.ws, C_TMO)); acquire_all(); }
    __syncthreads();
  });
  const int tid = otid(); const int lane = tid & 63, wid = tid >> 6, wm = wid >> 1, wn = wid & 1, fr = lane & 15, g = lane >> 4;
  f32x4* ex = (f32x4*)smem;
  if (wn == 1) {
#pragma unroll
    for (int j = 0; j < BN / 32; ++j) ex[(wm * (BN / 32) + j) * 64 + lane] = acc[0][j];
  }
  __syncthreads();
  if (wn == 0) {
    const __amdgpu_buffer_rsrc_t rg = __builtin_amdgcn_make_buffer_rsrc(a.ws + WS_G, 0, S_ * F_ * 2, 0x00020000);
#pragma unroll
    for (int j = 0; j < BN / 32; j += 2) {
      v2u pk[2];
#pragma unroll
      for (int jj = 0; jj < 2; ++jj) {
        f32x4 up = ex[(wm * (BN / 32) + j + jj) * 64 + lane];
        f32x4 o;
#pragma unroll
        for (int r = 0; r < 4; ++r) {
          float gt = rbf(acc[0][j + jj][r]);
          float sg = rbf(gt / (1.f + __expf(-gt)));
          o[r] = sg * rbf(up[r]);
        }
        pk[jj] = pk4(o);
      }
      v4u v = pair16(pk[0], pk[1]);
      int tok = m0 + wm * (BM / 2) + fr, f = f0 + (j + (g & 1)) * 16 + (g >> 1) * 8;
      __builtin_amdgcn_raw_buffer_store_b128(v, rg, (tok * F_ + f) * 2, 0, 16);
    }
  }
  __syncthreads();
}

__device__ __forceinline__ void task_f2(const MegaArgs& a, char* smem, int s, int nb) {
  constexpr int BM = 32, BN = 64;
  PGen P; P.A = (const u16*)(a.ws + WS_G); P.lda = F_; P.W = a.w2; P.W2 = nullptr; P.K = F_; P.ldb = D_;
  const int m0 = s * BM, n0 = nb * BN;
  f32x4 acc[BM / 32][BN / 32];
  gemm_tile_dma<BM, BN, 24, 8, true>(P, m0, n0, smem, acc, [&]() {
    if (threadIdx.x == 0) { spin_ge(ctl(a.ws, C_FS + s), 24u, ctl(a.ws, C_TMO)); acquire_all(); }
    __syncthreads();
  });
  const int tid = otid(); const int lane = tid & 63, wid = tid >> 6, wm = wid >> 1, wn = wid & 1, fr = lane & 15, g = lane >> 4;
  const u16* x1 = (const u16*)(a.ws + WS_X1);
  const float* gb = (const float*)(a.ws + WS_MOD) + 5 * D_;
#pragma unroll
  for (int j = 0; j < BN / 32; ++j) {
    int tok = m0 + wm * (BM / 2) + fr, c = n0 + wn * (BN / 2) + j * 16 + 4 * g;
    v2u xv = *(const v2u*)(x1 + (size_t)tok * D_ + c);
    float xo[4] = {bflo(xv[0]), bfhi(xv[0]), bflo(xv[1]), bfhi(xv[1])};
    f32x4 o;
#pragma unroll
    for (int r = 0; r < 4; ++r) o[r] = xo[r] + rbf(gb[c + r] * rbf(acc[0][j][r]));
    *(v2u*)(a.out + (size_t)tok * D_ + c) = pk4(o);
  }
}

constexpr int NP_S = 8 + 24 + 10;
constexpr int D1_ = 8, D2_ = 10;
constexpr int NT2_ = (NSEG_ + D2_) * NP_S;
__device__ __forceinline__ void worker_role(const MegaArgs& a, char* smem, const int x, const int j) {
  unsigned* tmo = ctl(a.ws, C_TMO);
  {
    const int nh = (x == 3) ? 4 : 7, h0 = (x == 3) ? 0 : 4 + (x - 4) * 7, nw = (x == 3) ? 16 : 32;
    const int bq0 = 112 + ((x == 3) ? 0 : 4 + (x - 4) * 11), nbx = (x == 3) ? 4 : 11;
    const int nsplit = 6 * nh + nbx;
    for (int e = j; e < nsplit + (NMT_ - 2) * nh; e += nw) {
      if (e >= 2 * nh && e < 2 * nh + nbx) {
        task_b(a, smem, bq0 + e - 2 * nh);
      } else if (e < nsplit) {
        const int e2 = e < 2 * nh ? e : e - nbx, row = e2 / nh, h = h0 + e2 % nh;
        const int mt = (row == 2 || row == 3 || row == 5) ? 1 : 0;
        if (row == 0 || row == 2) { task_qkvu<6, 3>(a, smem, mt, h); publish(ctl(a.ws, C_K + mt)); }
        else if (row == 1 || row == 3) { task_qkvu<3, 3>(a, smem, mt, h); publish(ctl(a.ws, C_K + mt)); }
        else { task_qkvu<0, 3>(a, smem, mt, h); publish(ctl(a.ws, C_Q0 + mt)); }
      } else {
        const int mt = 2 + (e - nsplit) / nh, h = h0 + (e - nsplit) % nh;
        TSTAMP(mt * 32 + h, 0); TSTAMP(mt * 32 + h, 1);
        task_qkvu<0, 9>(a, smem, mt, h);
        publish(ctl(a.ws, C_K + mt));
        TSTAMP(mt * 32 + h, 2);
      }
    }
  }
  volatile unsigned* lw = (volatile unsigned*)smem;
  for (;;) {
    __syncthreads();
    if (threadIdx.x == 0) lw[0] = __hip_atomic_fetch_add(ctl(a.ws, C_QUEUE), 1u, __ATOMIC_RELAXED, __HIP_MEMORY_SCOPE_AGENT);
    __syncthreads();
    const int t = __builtin_amdgcn_readfirstlane((int)lw[0]);
    __syncthreads();
    if (t >= NT2_) break;
    const int r = t % NP_S;
    const int s = t / NP_S - (r < 8 ? 0 : (r < 32 ? D1_ : D2_));
    if (s < 0 || s >= NSEG_) continue;
    TSTAMP(512 + s * NP_S + r, 0);
    if (r < 8) {
      const int hq = r;
      if (threadIdx.x == 0) { if (s < 8) spin_ge(ctl(a.ws, C_Q0 + (s >> 2)), 32u, tmo); spin_ge(ctl(a.ws, C_K + (s >> 2)), s < 8 ? 64u : 32u, tmo); acquire_all(); }
      __syncthreads();
      TSTAMP(512 + s * NP_S + r, 1);
      task_x(a, smem, s, hq);
      publish(ctl(a.ws, C_T + s));
      if (threadIdx.x == 0) { spin_ge(ctl(a.ws, C_T + s), 8u, tmo); acquire_all(); }
      __syncthreads();
      TSTAMP(512 + s * NP_S + r, 3);
      task_xc(a, smem, s, hq);
      publish(ctl(a.ws, C_XS + s));
    } else if (r < 32) {
      TSTAMP(512 + s * NP_S + r, 1);
      task_f1(a, smem, s, r - 8);
      publish(ctl(a.ws, C_FS + s));
    } else {
      TSTAMP(512 + s * NP_S + r, 1);
      task_f2(a, smem, s, r - 32);
      __syncthreads();
    }
    TSTAMP(512 + s * NP_S + r, 2);
  }
}

__global__ void __launch_bounds__(256, 1) k_mega(MegaArgs a) {
  extern __shared__ __attribute__((aligned(16))) char smem[];
  const int x = blockIdx.x & 7, j = blockIdx.x >> 3;
  if (x < 3 || (x == 3 && j < 16)) {
    const int cidx = (x < 3) ? x * 32 + j : 96 + j;
    if (cidx < H_) score_role(a, smem, cidx);
    else u_role(a, smem, cidx);
  } else {
    worker_role(a, smem, x, x == 3 ? j - 16 : j);
  }
}

extern "C" void kernel_launch(void* const* d_in, const int* in_sizes, int n_in,
                              void* d_out, int out_size, void* d_ws, size_t ws_size,
                              hipStream_t stream) {
  const u16* x = (const u16*)d_in[0];
  const u16* mem0 = (const u16*)d_in[1];
  const u16* Wq = (const u16*)d_in[2];
  const u16* Wk = (const u16*)d_in[3];
  const u16* Wv = (const u16*)d_in[4];
  const u16* Wo = (const u16*)d_in[5];
  const u16* w1 = (const u16*)d_in[8];
  const u16* w3 = (const u16*)d_in[9];
  const u16* w2 = (const u16*)d_in[10];
  const u16* c = (const u16*)d_in[11];
  const u16* wmod = (const u16*)d_in[12];
  char* ws = (char*)d_ws;
  float* mod = (float*)(ws + WS_MOD);

  (void)hipFuncSetAttribute((const void*)k_mega, hipFuncAttributeMaxDynamicSharedMemorySize, MEGA_LDS);

  k_pre<<<240, 256, 0, stream>>>(c, wmod, mem0, ws);
  k_ln<<<S_ / 4, 256, 0, stream>>>(x, mod + 0 * D_, mod + 1 * D_, (u16*)(ws + WS_H1));
  MegaArgs ma; ma.x = x; ma.Wq = Wq; ma.Wk = Wk; ma.Wv = Wv; ma.Wo = Wo; ma.w1 = w1; ma.w3 = w3; ma.w2 = w2; ma.ws = ws; ma.out = (u16*)d_out;
  k_mega<<<NBLK_, 256, MEGA_LDS, stream>>>(ma);
}
```
